```python
import torch

def _softmax(x, axis=-1):
    return torch.softmax(x, dim=axis)
def _gelu(x):
    return torch.nn.functional.gelu(x, approximate="tanh")
def _silu(x):
    return torch.nn.functional.silu(x)
def _sigmoid(x):
    return torch.sigmoid(x)
def _layernorm(x, axis=-1, eps=1e-5):
    m = x.mean(dim=axis, keepdim=True)
    v = x.var(dim=axis, keepdim=True, unbiased=False)
    return (x - m) / torch.sqrt(v + eps)
def _scatter_add(tgt, idx, src, axis=0):
    return tgt.clone().index_add_(axis, idx, src)
def _segment_sum(src, seg, n):
    out = torch.zeros((n,) + src.shape[1:], dtype=src.dtype)
    return out.index_add_(0, seg, src)
def _pad(x, pw):
    flat = tuple(v for pair in reversed(pw) for v in pair)
    return torch.nn.functional.pad(x, flat)

kernel_name = 'parallel_sum__linear_kernel_hedgehog__glu_gated_reglu__prompt'

def setup_inputs():

    _w = (lambda a: (lambda t: t.to(torch.bfloat16) if t.is_floating_point() else t)(torch.as_tensor(a)))
    def _A_setup():
        B, S, D, H = 1, 2048, 512, 32
        Dh = D // H
        return {
            "x": _w((lambda *s: torch.randn(*s, dtype=torch.bfloat16) * 0.05)(B, S, D)),
            "wqkv": _w((lambda *s: torch.randn(*s, dtype=torch.bfloat16) * 0.2)(D, 3 * D)),
            "w_phi": _w((lambda *s: torch.randn(*s, dtype=torch.bfloat16) * 0.2)(H, Dh, Dh) * (Dh ** -0.5)),
            "wo": _w((lambda *s: torch.randn(*s, dtype=torch.bfloat16) * 0.05)(D, D)),
        }
    _A_in = _A_setup()

    def _B_setup():
        B, S, D, F = 1, 2048, 512, 1792
        return {
            "x": _w((lambda *s: torch.randn(*s, dtype=torch.bfloat16) * 0.05)(B, S, D)),
            "W_gate": _w((lambda *s: torch.randn(*s, dtype=torch.bfloat16) * 0.4)(D, F) * (2.0 / D) ** 0.5),
            "W_up": _w((lambda *s: torch.randn(*s, dtype=torch.bfloat16) * 0.4)(D, F) * (2.0 / D) ** 0.5),
            "W_down": _w((lambda *s: torch.randn(*s, dtype=torch.bfloat16) * 0.4)(F, D) * (1.0 / F) ** 0.5),
        }
    _B_in = _B_setup()

    return {
        "A_x": _A_in["x"],
        "A_wqkv": _A_in["wqkv"],
        "A_w_phi": _A_in["w_phi"],
        "A_wo": _A_in["wo"],
        "B_x": _B_in["x"],
        "B_W_gate": _B_in["W_gate"],
        "B_W_up": _B_in["W_up"],
        "B_W_down": _B_in["W_down"],
    }

def reference(A_x, A_wqkv, A_w_phi, A_wo, B_x, B_W_gate, B_W_up, B_W_down):
    def _A_ref(x, wqkv, w_phi, wo):
        B, S, D = x.shape
        H = 32; Dh = D // H
        qkv = (lambda x, *d: x.permute(*d))((x @ wqkv).reshape(B, S, 3, H, Dh), 2, 0, 3, 1, 4)
        q, k, v = qkv[0], qkv[1], qkv[2]
        pq = torch.einsum("bhsd,hdf->bhsf", q, w_phi)
        pk = torch.einsum("bhsd,hdf->bhsf", k, w_phi)
        phi_q = torch.concatenate([_softmax(pq, axis=-1), _softmax(-pq, axis=-1)], axis=-1)
        phi_k = torch.concatenate([_softmax(pk, axis=-1), _softmax(-pk, axis=-1)], axis=-1)
        kv = torch.einsum("bhsf,bhsd->bhfd", phi_k, v)
        z  = torch.einsum("bhsf->bhf", phi_k)
        num = torch.einsum("bhqf,bhfd->bhqd", phi_q, kv)
        den = torch.einsum("bhqf,bhf->bhq", phi_q, z)[..., None] + 1e-6
        o = num / den
        return (lambda x, *d: x.permute(*d))(o, 0, 2, 1, 3).reshape(B, S, D) @ wo

    def _B_ref(x, W_gate, W_up, W_down):
        gate = torch.einsum("bsd,df->bsf", x, W_gate)
        up = torch.einsum("bsd,df->bsf", x, W_up)
        gate = gate * (lambda x, dt: x.to(dt))(gate > 0, gate.dtype)
        h = gate * up
        return torch.einsum("bsf,fd->bsd", h, W_down)

    x = A_x
    xn = _layernorm(x, axis=-1)
    return x + _A_ref(xn, A_wqkv, A_w_phi, A_wo) + _B_ref(xn, B_W_gate, B_W_up, B_W_down)
```

```cpp
#include <hip/hip_runtime.h>
#include <hip/hip_bf16.h>
#include <stdint.h>

typedef __attribute__((ext_vector_type(8))) short s16x8;
typedef __attribute__((ext_vector_type(4))) short s16x4;
typedef __attribute__((ext_vector_type(4))) float f32x4;
typedef __attribute__((address_space(3))) s16x4 lds_s16x4;

#define S_LEN 2048
#define DM 512
#define FF 1792
#define NH 32
#define KVP 544

#define WAIT_V(n) asm volatile("s_waitcnt vmcnt(" #n ")" ::: "memory")
#define BAR() __builtin_amdgcn_s_barrier()

__device__ __forceinline__ float b2f(short v) { return __uint_as_float(((unsigned)(unsigned short)v) << 16); }
__device__ __forceinline__ short f2b(float f) {
  __bf16 h = (__bf16)f;
  return __builtin_bit_cast(short, h);
}
__device__ __forceinline__ float rb(float f) { return b2f(f2b(f)); }

__device__ __forceinline__ void glds16(const void* g, void* l) {
  __builtin_amdgcn_global_load_lds((const unsigned*)g, (unsigned*)l, 16, 0, 0);
}
__device__ __forceinline__ s16x4 tr_read(const void* l) {
  return __builtin_amdgcn_ds_read_tr16_b64_v4i16((lds_s16x4*)l);
}

__device__ __forceinline__ int swz128(int k) { return (((k >> 1) & 1) << 1) | (((k >> 3) & 1) << 2); }
__device__ __forceinline__ int swz256(int k) { return ((k & 3) | (((k >> 3) & 1) << 2)) << 1; }

template <int NT>
__device__ __forceinline__ void load_ln(const short* __restrict__ x, int row0, int lane, s16x8 (&a)[NT][16]) {
  const int r = lane & 15, g = lane >> 4;
#pragma unroll
  for (int t = 0; t < NT; ++t) {
    const short* p = x + (size_t)(row0 + 16 * t + r) * DM + 8 * g;
#pragma unroll
    for (int s = 0; s < 16; ++s) a[t][s] = *(const s16x8*)(p + 32 * s);
  }
#pragma unroll
  for (int t = 0; t < NT; ++t) {
    float sum = 0.f;
#pragma unroll
    for (int s = 0; s < 16; ++s)
#pragma unroll
      for (int j = 0; j < 8; ++j) sum += b2f(a[t][s][j]);
    sum += __shfl_xor(sum, 16);
    sum += __shfl_xor(sum, 32);
    const float mean = sum * (1.0f / DM);
    float sq = 0.f;
#pragma unroll
    for (int s = 0; s < 16; ++s)
#pragma unroll
      for (int j = 0; j < 8; ++j) {
        float d = b2f(a[t][s][j]) - mean;
        sq += d * d;
      }
    sq += __shfl_xor(sq, 16);
    sq += __shfl_xor(sq, 32);
    const float mb = rb(mean);
    const float vb = rb(sq * (1.0f / DM));
    const float sd = rb(sqrtf(rb(vb + 1e-5f)));
#pragma unroll
    for (int s = 0; s < 16; ++s)
#pragma unroll
      for (int j = 0; j < 8; ++j) a[t][s][j] = f2b(rb(b2f(a[t][s][j]) - mb) / sd);
  }
}

__global__ __launch_bounds__(512, 2) void k1a(const short* __restrict__ x, const short* __restrict__ wqkv,
                                              const short* __restrict__ wphi, float* __restrict__ part) {
  __shared__ __attribute__((aligned(16))) char smem[65536];
  const int tid = threadIdx.x, lane = tid & 63, w = __builtin_amdgcn_readfirstlane(tid >> 6);
  const int bid = blockIdx.x, xcd = bid & 7, idx = bid >> 3;
  const int mi = (xcd & 3) * 4 + (idx & 3), j = (xcd >> 2) * 8 + (idx >> 2);
#pragma unroll
  for (int i = 0; i < 8; ++i) {
    int k = (w * 8 + i) * 8 + (lane >> 3);
    int c = (lane & 7) ^ swz128(k);
    int col = (c < 4) ? (512 + 32 * j + 8 * c) : (1024 + 32 * j + 8 * (c - 4));
    glds16(wqkv + (size_t)k * 1536 + col, smem + (w * 8 + i) * 1024);
  }
  s16x8 a[1][16];
  load_ln<1>(x, mi * 128 + w * 16, lane, a);
  WAIT_V(0);
  __syncthreads();
  f32x4 acc[4] = {};
  const int r = lane & 15, g = lane >> 4, q = r >> 2, p = r & 3;
#pragma unroll
  for (int s = 0; s < 16; ++s) {
#pragma unroll
    for (int n = 0; n < 4; ++n) {
      int k0 = 32 * s + 8 * g + q, k1 = k0 + 4;
      int c = 2 * n + (p >> 1);
      s16x4 lo = tr_read(smem + k0 * 128 + ((c ^ swz128(k0)) << 4) + 8 * (p & 1));
      s16x4 hi = tr_read(smem + k1 * 128 + ((c ^ swz128(k1)) << 4) + 8 * (p & 1));
      s16x8 b = __builtin_shufflevector(lo, hi, 0, 1, 2, 3, 4, 5, 6, 7);
      if (n < 2) acc[n] = __builtin_amdgcn_mfma_f32_16x16x32_bf16(b, a[0][s], acc[n], 0, 0, 0);
      else acc[n] = __builtin_amdgcn_mfma_f32_16x16x32_bf16(a[0][s], b, acc[n], 0, 0, 0);
    }
  }
  __syncthreads();
  float* red = (float*)smem;
#pragma unroll
  for (int hh = 0; hh < 2; ++hh) {
    const int head = 2 * j + hh;
    s16x4 ka, wb, va;
#pragma unroll
    for (int t = 0; t < 4; ++t) {
      ka[t] = f2b(acc[hh][t]);
      va[t] = f2b(acc[2 + hh][t]);
      wb[t] = wphi[head * 256 + (4 * g + t) * 16 + r];
    }
    f32x4 pk = {};
    pk = __builtin_amdgcn_mfma_f32_16x16x16bf16_1k(ka, wb, pk, 0, 0, 0);
    s16x4 php, phn;
    float zp = 0.f, zn = 0.f;
#pragma unroll
    for (int t = 0; t < 4; ++t) {
      float v = rb(pk[t]);
      float mx = v, mn = v;
#pragma unroll
      for (int o = 1; o < 16; o <<= 1) {
        mx = fmaxf(mx, __shfl_xor(mx, o));
        mn = fminf(mn, __shfl_xor(mn, o));
      }
      float ep = __expf(v - mx), en = __expf(mn - v);
      float sp = ep, sn = en;
#pragma unroll
      for (int o = 1; o < 16; o <<= 1) {
        sp += __shfl_xor(sp, o);
        sn += __shfl_xor(sn, o);
      }
      php[t] = f2b(ep / sp);
      phn[t] = f2b(en / sn);
      zp += b2f(php[t]);
      zn += b2f(phn[t]);
    }
    zp += __shfl_xor(zp, 16); zp += __shfl_xor(zp, 32);
    zn += __shfl_xor(zn, 16); zn += __shfl_xor(zn, 32);
    f32x4 kp = {}, kn = {};
    kp = __builtin_amdgcn_mfma_f32_16x16x16bf16_1k(va, php, kp, 0, 0, 0);
    kn = __builtin_amdgcn_mfma_f32_16x16x16bf16_1k(va, phn, kn, 0, 0, 0);
    float* rr = red + (w * 2 + hh) * KVP;
#pragma unroll
    for (int t = 0; t < 4; ++t) {
      rr[r * 16 + 4 * g + t] = kp[t];
      rr[(16 + r) * 16 + 4 * g + t] = kn[t];
    }
    if (g == 0) {
      rr[512 + r] = zp;
      rr[512 + 16 + r] = zn;
    }
  }
  __syncthreads();
  for (int e = tid; e < 2 * KVP; e += 512) {
    float s = 0.f;
#pragma unroll
    for (int ww = 0; ww < 8; ++ww) s += red[ww * 2 * KVP + e];
    part[((size_t)mi * NH + 2 * j) * KVP + e] = s;
  }
}

__device__ __forceinline__ void k1b_epilogue(f32x4 (&acc)[2][8], int r, int g, int mi, int j, int wm, int wn,
                                             const short* __restrict__ wphi, const float* kvs,
                                             short* __restrict__ o_out, short* __restrict__ h_out) {
#pragma unroll
  for (int m = 0; m < 2; ++m) {
    const size_t row = (size_t)mi * 128 + wm * 32 + m * 16 + r;
    if (wn == 0) {
#pragma unroll
      for (int hh = 0; hh < 2; ++hh) {
        const int head = 2 * j + hh;
        const float* kv = kvs + hh * KVP;
        s16x4 qb, wa, kap, kan;
#pragma unroll
        for (int t = 0; t < 4; ++t) {
          qb[t] = f2b(acc[m][hh][t]);
          wa[t] = wphi[head * 256 + (4 * g + t) * 16 + r];
          kap[t] = f2b(kv[(4 * g + t) * 16 + r]);
          kan[t] = f2b(kv[(16 + 4 * g + t) * 16 + r]);
        }
        f32x4 pq = {};
        pq = __builtin_amdgcn_mfma_f32_16x16x16bf16_1k(wa, qb, pq, 0, 0, 0);
        float v[4], mx = -3.0e38f, mn = 3.0e38f;
#pragma unroll
        for (int t = 0; t < 4; ++t) { v[t] = rb(pq[t]); mx = fmaxf(mx, v[t]); mn = fminf(mn, v[t]); }
        mx = fmaxf(mx, __shfl_xor(mx, 16)); mx = fmaxf(mx, __shfl_xor(mx, 32));
        mn = fminf(mn, __shfl_xor(mn, 16)); mn = fminf(mn, __shfl_xor(mn, 32));
        float ep[4], en[4], sp = 0.f, sn = 0.f;
#pragma unroll
        for (int t = 0; t < 4; ++t) { ep[t] = __expf(v[t] - mx); en[t] = __expf(mn - v[t]); sp += ep[t]; sn += en[t]; }
        sp += __shfl_xor(sp, 16); sp += __shfl_xor(sp, 32);
        sn += __shfl_xor(sn, 16); sn += __shfl_xor(sn, 32);
        s16x4 php, phn;
        float den = 0.f;
#pragma unroll
        for (int t = 0; t < 4; ++t) {
          php[t] = f2b(ep[t] / sp); phn[t] = f2b(en[t] / sn);
          den += b2f(php[t]) * kv[512 + 4 * g + t] + b2f(phn[t]) * kv[512 + 16 + 4 * g + t];
        }
        den += __shfl_xor(den, 16); den += __shfl_xor(den, 32);
        den = rb(rb(den) + 1e-6f);
        f32x4 num = {};
        num = __builtin_amdgcn_mfma_f32_16x16x16bf16_1k(kap, php, num, 0, 0, 0);
        num = __builtin_amdgcn_mfma_f32_16x16x16bf16_1k(kan, phn, num, 0, 0, 0);
        s16x4 ov;
#pragma unroll
        for (int t = 0; t < 4; ++t) ov[t] = f2b(rb(num[t]) / den);
        *(s16x4*)(o_out + row * DM + head * 16 + 4 * g) = ov;
      }
#pragma unroll
      for (int n = 0; n < 3; ++n) {
        s16x4 hv;
#pragma unroll
        for (int t = 0; t < 4; ++t) hv[t] = f2b(fmaxf(rb(acc[m][2 + n][t]), 0.f) * rb(acc[m][5 + n][t]));
        *(s16x4*)(h_out + row * FF + 112 * j + 16 * n + 4 * g) = hv;
      }
    } else {
#pragma unroll
      for (int n = 0; n < 4; ++n) {
        s16x4 hv;
#pragma unroll
        for (int t = 0; t < 4; ++t) hv[t] = f2b(fmaxf(rb(acc[m][n][t]), 0.f) * rb(acc[m][4 + n][t]));
        *(s16x4*)(h_out + row * FF + 112 * j + 48 + 16 * n + 4 * g) = hv;
      }
    }
  }
}


#define K1B_NST 8
__global__ __launch_bounds__(512, 2) void k1b(const short* __restrict__ x, const short* __restrict__ wqkv,
                                              const short* __restrict__ wphi, const short* __restrict__ wg,
                                              const short* __restrict__ wu, const float* __restrict__ part,
                                              short* __restrict__ o_out, short* __restrict__ h_out) {
  __shared__ __attribute__((aligned(16))) char smem[K1B_NST * 16384 + 2 * KVP * 4];
  const int tid = threadIdx.x, lane = tid & 63, w = __builtin_amdgcn_readfirstlane(tid >> 6);
  const int bid = blockIdx.x, xcd = bid & 7, idx = bid >> 3;
  const int mi = (xcd & 3) * 4 + (idx & 3), j = (xcd >> 2) * 8 + (idx >> 2);
  const int wm = w >> 1, wn = w & 1;
  const short* src[2];
  int sstride[2];
#pragma unroll
  for (int i = 0; i < 2; ++i) {
    int kr = (w * 2 + i) * 2 + (lane >> 5);
    int c = (lane & 31) ^ swz256(kr);
    const short* b; int st;
    if (c < 4) { b = wqkv + 32 * j + 8 * c; st = 1536; }
    else if (c < 10) { b = wg + 112 * j + 8 * (c - 4); st = FF; }
    else if (c < 16) { b = wu + 112 * j + 8 * (c - 10); st = FF; }
    else if (c < 24) { b = wg + 112 * j + 48 + 8 * (c - 16); st = FF; }
    else { b = wu + 112 * j + 48 + 8 * (c - 24); st = FF; }
    src[i] = b + (size_t)kr * st;
    sstride[i] = 32 * st;
  }
  auto stage = [&](int t) {
    char* dst = smem + (t % K1B_NST) * 16384 + (w * 2) * 1024;
    glds16(src[0] + (size_t)t * sstride[0], dst);
    glds16(src[1] + (size_t)t * sstride[1], dst + 1024);
  };
#pragma unroll
  for (int t = 0; t < K1B_NST - 1; ++t) stage(t);
  float* kvs = (float*)(smem + K1B_NST * 16384);
  for (int e = tid; e < 2 * KVP; e += 512) {
    float s = 0.f;
#pragma unroll
    for (int i = 0; i < 16; ++i) s += part[((size_t)i * NH + 2 * j) * KVP + e];
    kvs[e] = rb(s);
  }
  s16x8 a[2][16];
  load_ln<2>(x, mi * 128 + wm * 32, lane, a);
  f32x4 acc[2][8] = {};
  const int r = lane & 15, g = lane >> 4, q = r >> 2, p = r & 3;
#pragma unroll
  for (int s = 0; s < 16; ++s) {
    if (s < 16 - (K1B_NST - 1)) { WAIT_V(12); } else { WAIT_V(0); }
    BAR();
    if (s + K1B_NST - 1 < 16) stage(s + K1B_NST - 1);
    const char* buf = smem + (s % K1B_NST) * 16384;
#pragma unroll
    for (int n = 0; n < 8; ++n) {
      int k0 = 8 * g + q, k1 = k0 + 4;
      int c = wn * 16 + 2 * n + (p >> 1);
      s16x4 lo = tr_read(buf + k0 * 512 + ((c ^ swz256(k0)) << 4) + 8 * (p & 1));
      s16x4 hi = tr_read(buf + k1 * 512 + ((c ^ swz256(k1)) << 4) + 8 * (p & 1));
      s16x8 b = __builtin_shufflevector(lo, hi, 0, 1, 2, 3, 4, 5, 6, 7);
#pragma unroll
      for (int m = 0; m < 2; ++m) acc[m][n] = __builtin_amdgcn_mfma_f32_16x16x32_bf16(b, a[m][s], acc[m][n], 0, 0, 0);
    }
  }
  int tz = threadIdx.x;
  asm volatile("" : "+v"(tz));
  k1b_epilogue(acc, tz & 15, (tz & 63) >> 4, mi, j, wm, wn, wphi, kvs, o_out, h_out);
}

#define K3_NST 8
__global__ __launch_bounds__(256, 1) void k3(const short* __restrict__ x, const short* __restrict__ o_in,
                                             const short* __restrict__ h_in, const short* __restrict__ wo,
                                             const short* __restrict__ wd, short* __restrict__ out) {
  __shared__ __attribute__((aligned(16))) char smem[K3_NST * 16384];
  const int tid = threadIdx.x, lane = tid & 63, w = __builtin_amdgcn_readfirstlane(tid >> 6);
  const int bid = blockIdx.x, xcd = bid & 7, idx = bid >> 3;
  const int pm = xcd * 4 + (idx & 3), pn = idx >> 2;
  const int wm = w >> 1, wn = w & 1;
  const int lr = lane >> 3, lc = lane & 7;
  auto stage = [&](int t) {
    char* dst = smem + (t % K3_NST) * 16384 + (w * 2) * 1024;
#pragma unroll
    for (int i = 0; i < 2; ++i) {
      int rr = (w * 2 + i) * 8 + lr;
      const short* ga = (t < 8) ? (o_in + (size_t)(pm * 64 + rr) * DM + 64 * t + 8 * (lc ^ (rr & 7)))
                                : (h_in + (size_t)(pm * 64 + rr) * FF + 64 * (t - 8) + 8 * (lc ^ (rr & 7)));
      glds16(ga, dst + i * 1024);
    }
#pragma unroll
    for (int i = 0; i < 2; ++i) {
      int rr = (w * 2 + i) * 8 + lr;
      const short* gb = (t < 8) ? (wo + (size_t)(64 * t + rr) * DM) : (wd + (size_t)(64 * (t - 8) + rr) * DM);
      glds16(gb + pn * 64 + 8 * (lc ^ swz128(rr)), dst + 8192 + i * 1024);
    }
  };
#pragma unroll
  for (int t = 0; t < K3_NST - 1; ++t) stage(t);
  f32x4 acc[2][2] = {};
  const int r = lane & 15, g = lane >> 4, q = r >> 2, p = r & 3;
  for (int t = 0; t < 36; ++t) {
    if (t < 36 - (K3_NST - 1)) { WAIT_V(24); } else { WAIT_V(0); }
    BAR();
    if (t + K3_NST - 1 < 36) stage(t + K3_NST - 1);
    const char* bufa = smem + (t % K3_NST) * 16384;
    const char* bufb = bufa + 8192;
#pragma unroll
    for (int kk = 0; kk < 2; ++kk) {
      s16x8 af[2], bf[2];
#pragma unroll
      for (int m = 0; m < 2; ++m) {
        int rr = wm * 32 + m * 16 + r;
        af[m] = *(const s16x8*)(bufa + rr * 128 + (((kk * 4 + g) ^ (rr & 7)) << 4));
      }
#pragma unroll
      for (int n = 0; n < 2; ++n) {
        int k0 = kk * 32 + 8 * g + q, k1 = k0 + 4;
        int c = wn * 4 + 2 * n + (p >> 1);
        s16x4 lo = tr_read(bufb + k0 * 128 + ((c ^ swz128(k0)) << 4) + 8 * (p & 1));
        s16x4 hi = tr_read(bufb + k1 * 128 + ((c ^ swz128(k1)) << 4) + 8 * (p & 1));
        bf[n] = __builtin_shufflevector(lo, hi, 0, 1, 2, 3, 4, 5, 6, 7);
      }
#pragma unroll
      for (int m = 0; m < 2; ++m)
#pragma unroll
        for (int n = 0; n < 2; ++n) acc[m][n] = __builtin_amdgcn_mfma_f32_16x16x32_bf16(bf[n], af[m], acc[m][n], 0, 0, 0);
    }
  }
#pragma unroll
  for (int m = 0; m < 2; ++m)
#pragma unroll
    for (int n = 0; n < 2; ++n) {
      size_t off = (size_t)(pm * 64 + wm * 32 + m * 16 + r) * DM + pn * 64 + wn * 32 + n * 16 + 4 * g;
      s16x4 xv = *(const s16x4*)(x + off);
      s16x4 ov;
#pragma unroll
      for (int t = 0; t < 4; ++t) ov[t] = f2b(b2f(xv[t]) + acc[m][n][t]);
      *(s16x4*)(out + off) = ov;
    }
}

extern "C" void kernel_launch(void* const* d_in, const int* in_sizes, int n_in, void* d_out, int out_size,
                              void* d_ws, size_t ws_size, hipStream_t stream) {
  const short* x = (const short*)d_in[0];
  const short* wqkv = (const short*)d_in[1];
  const short* wphi = (const short*)d_in[2];
  const short* wo = (const short*)d_in[3];
  const short* wg = (const short*)d_in[5];
  const short* wu = (const short*)d_in[6];
  const short* wd = (const short*)d_in[7];
  char* ws = (char*)d_ws;
  float* part = (float*)ws;
  short* o_buf = (short*)(ws + (2 << 20));
  short* h_buf = (short*)(ws + (4 << 20));
  k1a<<<256, 512, 0, stream>>>(x, wqkv, wphi, part);
  k1b<<<256, 512, 0, stream>>>(x, wqkv, wphi, wg, wu, part, o_buf, h_buf);
  k3<<<256, 256, 0, stream>>>(x, o_buf, h_buf, wo, wd, (short*)d_out);
}
```

```cpp
#include <hip/hip_runtime.h>
#include <hip/hip_bf16.h>
#include <stdint.h>

typedef __attribute__((ext_vector_type(8))) short s16x8;
typedef __attribute__((ext_vector_type(4))) short s16x4;
typedef __attribute__((ext_vector_type(4))) float f32x4;
typedef __attribute__((ext_vector_type(2))) float f32x2;
typedef __attribute__((ext_vector_type(4))) unsigned u32x4;

#define S_LEN 2048
#define DM 512
#define FF 1792
#define NH 32
#define KVP 544

#define WAIT_V(n) asm volatile("s_waitcnt vmcnt(" #n ")" ::: "memory")
#define BAR() __builtin_amdgcn_s_barrier()

#define STAMP(i)
#define STAMP_DECL
#define STAMP_OUT(dbg)

__device__ __forceinline__ float b2f(short v) { return __uint_as_float(((unsigned)(unsigned short)v) << 16); }
__device__ __forceinline__ short f2b(float f) {
  __bf16 h = (__bf16)f;
  return __builtin_bit_cast(short, h);
}
__device__ __forceinline__ float rb(float f) { return b2f(f2b(f)); }

__device__ __forceinline__ void glds16(const void* g, void* l) {
  __builtin_amdgcn_global_load_lds((const unsigned*)g, (unsigned*)l, 16, 0, 0);
}
__device__ __forceinline__ unsigned lds_addr(const void* p) { return (unsigned)(size_t)p; }
#define TR2(lo, hi, a0, a1, off) \
  asm volatile("ds_read_b64_tr_b16 %0, %2 offset:%4\n\tds_read_b64_tr_b16 %1, %3 offset:%4" \
               : "=v"(lo), "=v"(hi) : "v"(a0), "v"(a1), "i"(off) : "memory")
#define WAIT_L0() do { asm volatile("s_waitcnt lgkmcnt(0)" ::: "memory"); __builtin_amdgcn_sched_barrier(0); } while (0)

__device__ __forceinline__ int swz128(int k) { return (((k >> 1) & 1) << 1) | (((k >> 3) & 1) << 2); }
__device__ __forceinline__ int swz256(int k) { return ((k & 3) | (((k >> 3) & 1) << 2)) << 1; }

__device__ __forceinline__ f32x2 unpack2(unsigned u) {
  f32x2 r;
  r[0] = __uint_as_float(u << 16);
  r[1] = __uint_as_float(u & 0xffff0000u);
  return r;
}

__device__ __forceinline__ void sw16(float v, float& a, float& b) {
  auto r = __builtin_amdgcn_permlane16_swap(__float_as_uint(v), __float_as_uint(v), false, false);
  a = __uint_as_float(r[0]); b = __uint_as_float(r[1]);
}
__device__ __forceinline__ void sw32(float v, float& a, float& b) {
  auto r = __builtin_amdgcn_permlane32_swap(__float_as_uint(v), __float_as_uint(v), false, false);
  a = __uint_as_float(r[0]); b = __uint_as_float(r[1]);
}
__device__ __forceinline__ float red_sum(float v) { float a, b; sw16(v, a, b); v = a + b; sw32(v, a, b); return a + b; }
__device__ __forceinline__ float red_max(float v) { float a, b; sw16(v, a, b); v = fmaxf(a, b); sw32(v, a, b); return fmaxf(a, b); }
__device__ __forceinline__ float red_min(float v) { float a, b; sw16(v, a, b); v = fminf(a, b); sw32(v, a, b); return fminf(a, b); }

__device__ __forceinline__ void ln_frag(s16x8 (&a)[16]) {
  f32x2 sum2 = {0.f, 0.f};
#pragma unroll
  for (int s = 0; s < 16; ++s) {
    u32x4 u = __builtin_bit_cast(u32x4, a[s]);
#pragma unroll
    for (int j = 0; j < 4; ++j) sum2 += unpack2(u[j]);
  }
  float sum = sum2[0] + sum2[1];
  sum = red_sum(sum);
  const float mean = sum * (1.0f / DM);
  const f32x2 mean2 = {mean, mean};
  f32x2 sq2 = {0.f, 0.f};
#pragma unroll
  for (int s = 0; s < 16; ++s) {
    u32x4 u = __builtin_bit_cast(u32x4, a[s]);
#pragma unroll
    for (int j = 0; j < 4; ++j) {
      f32x2 d = unpack2(u[j]) - mean2;
      sq2 += d * d;
    }
  }
  float sq = sq2[0] + sq2[1];
  sq = red_sum(sq);
  const float rs = 1.0f / rb(sqrtf(rb(rb(sq * (1.0f / DM)) + 1e-5f)));
  const f32x2 rs2 = {rs, rs};
  const float nb = -rb(mean) * rs;
  const f32x2 nb2 = {nb, nb};
#pragma unroll
  for (int s = 0; s < 16; ++s) {
    u32x4 u = __builtin_bit_cast(u32x4, a[s]);
#pragma unroll
    for (int j = 0; j < 4; ++j) {
      f32x2 v = unpack2(u[j]) * rs2 + nb2;
      a[s][2 * j] = f2b(v[0]);
      a[s][2 * j + 1] = f2b(v[1]);
    }
  }
}

__device__ __forceinline__ void dual_softmax(const f32x4& p, s16x4& php, s16x4& phn) {
  float v[4], mx = -3.0e38f, mn = 3.0e38f;
#pragma unroll
  for (int t = 0; t < 4; ++t) { v[t] = rb(p[t]); mx = fmaxf(mx, v[t]); mn = fminf(mn, v[t]); }
  mx = red_max(mx); mn = red_min(mn);
  float ep[4], en[4], sp = 0.f, sn = 0.f;
#pragma unroll
  for (int t = 0; t < 4; ++t) { ep[t] = __expf(v[t] - mx); en[t] = __expf(mn - v[t]); sp += ep[t]; sn += en[t]; }
  sp = red_sum(sp); sn = red_sum(sn);
  const float rp = 1.0f / sp, rn = 1.0f / sn;
#pragma unroll
  for (int t = 0; t < 4; ++t) { php[t] = f2b(ep[t] * rp); phn[t] = f2b(en[t] * rn); }
}

__global__ __launch_bounds__(512, 2) void k1a(const short* __restrict__ x, const short* __restrict__ wqkv,
                                              const short* __restrict__ wphi, float* __restrict__ part,
                                              short* __restrict__ xn) {
  __shared__ __attribute__((aligned(16))) char smem[65536];
  STAMP_DECL;
  STAMP(0);
  const int tid = threadIdx.x, lane = tid & 63, w = __builtin_amdgcn_readfirstlane(tid >> 6);
  const int bid = blockIdx.x, xcd = bid & 7, idx = bid >> 3;
  const int mi = (xcd & 3) * 4 + (idx & 3), j = (xcd >> 2) * 8 + (idx >> 2);
  const int r = lane & 15, g = lane >> 4, q = r >> 2, p = r & 3;
  s16x8 a[16];
  {
    const short* px = x + (size_t)(mi * 128 + w * 16 + r) * DM + 8 * g;
#pragma unroll
    for (int s = 0; s < 16; ++s) a[s] = *(const s16x8*)(px + 32 * s);
  }
#pragma unroll
  for (int i = 0; i < 8; ++i) {
    int k = (w * 8 + i) * 8 + (lane >> 3);
    int c = (lane & 7) ^ swz128(k);
    int col = (c < 4) ? (512 + 32 * j + 8 * c) : (1024 + 32 * j + 8 * (c - 4));
    glds16(wqkv + (size_t)k * 1536 + col, smem + (w * 8 + i) * 1024);
  }
  ln_frag(a);
  if (w == (j >> 1) && (r >> 3) == (j & 1)) {
    short* pn = xn + (size_t)(mi * 128 + w * 16 + r) * DM + 8 * g;
#pragma unroll
    for (int s = 0; s < 16; ++s) *(s16x8*)(pn + 32 * s) = a[s];
  }
  STAMP(1);
  WAIT_V(0);
  __syncthreads();
  STAMP(2);
  f32x4 acc[4] = {};
  {
    unsigned ad[4][2];
#pragma unroll
    for (int n = 0; n < 4; ++n) {
      int k0 = 8 * g + q, k1 = k0 + 4, c = 2 * n + (p >> 1);
      ad[n][0] = lds_addr(smem) + k0 * 128 + ((c ^ swz128(k0)) << 4) + 8 * (p & 1);
      ad[n][1] = lds_addr(smem) + k1 * 128 + ((c ^ swz128(k1)) << 4) + 8 * (p & 1);
    }
#pragma unroll
    for (int s = 0; s < 16; ++s) {
      s16x4 lo[4], hi[4];
#pragma unroll
      for (int n = 0; n < 4; ++n) TR2(lo[n], hi[n], ad[n][0], ad[n][1], s * 4096);
      WAIT_L0();
#pragma unroll
      for (int n = 0; n < 4; ++n) {
        s16x8 b = __builtin_shufflevector(lo[n], hi[n], 0, 1, 2, 3, 4, 5, 6, 7);
        if (n < 2) acc[n] = __builtin_amdgcn_mfma_f32_16x16x32_bf16(b, a[s], acc[n], 0, 0, 0);
        else acc[n] = __builtin_amdgcn_mfma_f32_16x16x32_bf16(a[s], b, acc[n], 0, 0, 0);
      }
    }
  }
  STAMP(3);
  __syncthreads();
  float* red = (float*)smem;
  s16x4 ident;
#pragma unroll
  for (int t = 0; t < 4; ++t) ident[t] = (4 * g + t == r) ? (short)0x3f80 : (short)0;
#pragma unroll
  for (int hh = 0; hh < 2; ++hh) {
    const int head = 2 * j + hh;
    s16x4 kb, wa, va;
#pragma unroll
    for (int t = 0; t < 4; ++t) {
      kb[t] = f2b(acc[hh][t]);
      va[t] = f2b(acc[2 + hh][t]);
      wa[t] = wphi[head * 256 + (4 * g + t) * 16 + r];
    }
    f32x4 pk = {};
    pk = __builtin_amdgcn_mfma_f32_16x16x16bf16_1k(wa, kb, pk, 0, 0, 0);
    s16x4 php, phn;
    dual_softmax(pk, php, phn);
    f32x4 tp = {}, tn = {};
    tp = __builtin_amdgcn_mfma_f32_16x16x16bf16_1k(php, ident, tp, 0, 0, 0);
    tn = __builtin_amdgcn_mfma_f32_16x16x16bf16_1k(phn, ident, tn, 0, 0, 0);
    s16x4 bp, bn;
    float zp = 0.f, zn = 0.f;
#pragma unroll
    for (int t = 0; t < 4; ++t) { bp[t] = f2b(tp[t]); bn[t] = f2b(tn[t]); zp += tp[t]; zn += tn[t]; }
    zp = red_sum(zp); zn = red_sum(zn);
    f32x4 kp = {}, kn = {};
    kp = __builtin_amdgcn_mfma_f32_16x16x16bf16_1k(va, bp, kp, 0, 0, 0);
    kn = __builtin_amdgcn_mfma_f32_16x16x16bf16_1k(va, bn, kn, 0, 0, 0);
    float* rr = red + (w * 2 + hh) * KVP;
    *(f32x4*)(rr + r * 16 + 4 * g) = kp;
    *(f32x4*)(rr + (16 + r) * 16 + 4 * g) = kn;
    if (g == 0) {
      rr[512 + r] = zp;
      rr[512 + 16 + r] = zn;
    }
  }
  __syncthreads();
  for (int e = tid; e < 2 * KVP; e += 512) {
    float s = 0.f;
#pragma unroll
    for (int ww = 0; ww < 8; ++ww) s += red[ww * 2 * KVP + e];
    part[((size_t)mi * NH + 2 * j) * KVP + e] = s;
  }
  STAMP(4);
  STAMP_OUT((unsigned long long*)(part + (4 << 20)));
}

__device__ __forceinline__ void k1b_epilogue(f32x4 (&acc)[2][8], int r, int g, int mi, int j, int wm, int wn,
                                             const short* wphi, const float* kvs,
                                             short* __restrict__ o_out, short* __restrict__ h_out) {
#pragma unroll
  for (int m = 0; m < 2; ++m) {
    const size_t row = (size_t)mi * 128 + wm * 32 + m * 16 + r;
    if (wn == 0) {
#pragma unroll
      for (int hh = 0; hh < 2; ++hh) {
        const int head = 2 * j + hh;
        const float* kv = kvs + hh * KVP;
        s16x4 qb, wa, kap, kan;
#pragma unroll
        for (int t = 0; t < 4; ++t) {
          qb[t] = f2b(acc[m][hh][t]);
          wa[t] = wphi[head * 256 + (4 * g + t) * 16 + r];
          kap[t] = f2b(kv[(4 * g + t) * 16 + r]);
          kan[t] = f2b(kv[(16 + 4 * g + t) * 16 + r]);
        }
        f32x4 pq = {};
        pq = __builtin_amdgcn_mfma_f32_16x16x16bf16_1k(wa, qb, pq, 0, 0, 0);
        s16x4 php, phn;
        dual_softmax(pq, php, phn);
        float den = 0.f;
#pragma unroll
        for (int t = 0; t < 4; ++t)
          den += b2f(php[t]) * kv[512 + 4 * g + t] + b2f(phn[t]) * kv[512 + 16 + 4 * g + t];
        den = red_sum(den);
        const float rd = 1.0f / rb(rb(den) + 1e-6f);
        f32x4 num = {};
        num = __builtin_amdgcn_mfma_f32_16x16x16bf16_1k(kap, php, num, 0, 0, 0);
        num = __builtin_amdgcn_mfma_f32_16x16x16bf16_1k(kan, phn, num, 0, 0, 0);
        s16x4 ov;
#pragma unroll
        for (int t = 0; t < 4; ++t) ov[t] = f2b(rb(num[t]) * rd);
        *(s16x4*)(o_out + row * DM + head * 16 + 4 * g) = ov;
      }
#pragma unroll
      for (int n = 0; n < 3; ++n) {
        s16x4 hv;
#pragma unroll
        for (int t = 0; t < 4; ++t) hv[t] = f2b(fmaxf(rb(acc[m][2 + n][t]), 0.f) * rb(acc[m][5 + n][t]));
        *(s16x4*)(h_out + row * FF + 112 * j + 16 * n + 4 * g) = hv;
      }
    } else {
#pragma unroll
      for (int n = 0; n < 4; ++n) {
        s16x4 hv;
#pragma unroll
        for (int t = 0; t < 4; ++t) hv[t] = f2b(fmaxf(rb(acc[m][n][t]), 0.f) * rb(acc[m][4 + n][t]));
        *(s16x4*)(h_out + row * FF + 112 * j + 48 + 16 * n + 4 * g) = hv;
      }
    }
  }
}

#define K1B_NST 4
__global__ __launch_bounds__(512, 2) void k1b(const short* __restrict__ xn, const short* __restrict__ wqkv,
                                              const short* __restrict__ wphi, const short* __restrict__ wg,
                                              const short* __restrict__ wu, const float* __restrict__ part,
                                              short* __restrict__ o_out, short* __restrict__ h_out) {
  __shared__ __attribute__((aligned(16))) char smem[K1B_NST * 16384 + 2 * KVP * 4];
  STAMP_DECL;
  STAMP(0);
  const int tid = threadIdx.x, lane = tid & 63, w = __builtin_amdgcn_readfirstlane(tid >> 6);
  const int bid = blockIdx.x, xcd = bid & 7, idx = bid >> 3;
  const int mi = (xcd & 3) * 4 + (idx & 3), j = (xcd >> 2) * 8 + (idx >> 2);
  const int wm = w >> 1, wn = w & 1;
  const int r = lane & 15, g = lane >> 4, q = r >> 2, p = r & 3;
  const short* src[2];
  int sstride[2];
#pragma unroll
  for (int i = 0; i < 2; ++i) {
    int kr = (w * 2 + i) * 2 + (lane >> 5);
    int c = (lane & 31) ^ swz256(kr);
    const short* b; int st;
    if (c < 4) { b = wqkv + 32 * j + 8 * c; st = 1536; }
    else if (c < 10) { b = wg + 112 * j + 8 * (c - 4); st = FF; }
    else if (c < 16) { b = wu + 112 * j + 8 * (c - 10); st = FF; }
    else if (c < 24) { b = wg + 112 * j + 48 + 8 * (c - 16); st = FF; }
    else { b = wu + 112 * j + 48 + 8 * (c - 24); st = FF; }
    src[i] = b + (size_t)kr * st;
    sstride[i] = 32 * st;
  }
  auto stage = [&](int t) {
    char* dst = smem + (t % K1B_NST) * 16384 + (w * 2) * 1024;
    glds16(src[0] + (size_t)t * sstride[0], dst);
    glds16(src[1] + (size_t)t * sstride[1], dst + 1024);
  };
  s16x8 a[2][16];
#pragma unroll
  for (int t = 0; t < 2; ++t) {
    const short* px = xn + (size_t)(mi * 128 + wm * 32 + 16 * t + r) * DM + 8 * g;
#pragma unroll
    for (int s = 0; s < 16; ++s) a[t][s] = *(const s16x8*)(px + 32 * s);
  }
  float* kvs = (float*)(smem + K1B_NST * 16384);
  for (int e = tid; e < 2 * KVP; e += 512) {
    float s = 0.f;
#pragma unroll
    for (int i = 0; i < 16; ++i) s += part[((size_t)i * NH + 2 * j) * KVP + e];
    kvs[e] = rb(s);
  }
  STAMP(1);
#pragma unroll
  for (int t = 0; t < K1B_NST - 1; ++t) stage(t);
  f32x4 acc[2][8] = {};
  unsigned ad[8][2];
#pragma unroll
  for (int n = 0; n < 8; ++n) {
    int k0 = 8 * g + q, k1 = k0 + 4, c = wn * 16 + 2 * n + (p >> 1);
    ad[n][0] = lds_addr(smem) + k0 * 512 + ((c ^ swz256(k0)) << 4) + 8 * (p & 1);
    ad[n][1] = lds_addr(smem) + k1 * 512 + ((c ^ swz256(k1)) << 4) + 8 * (p & 1);
  }
#pragma unroll
  for (int s = 0; s < 16; ++s) {
    if (s < 16 - (K1B_NST - 1)) { WAIT_V(4); } else { WAIT_V(0); }
    BAR();
    if (s + K1B_NST - 1 < 16) stage(s + K1B_NST - 1);
    s16x4 lo[8], hi[8];
#pragma unroll
    for (int n = 0; n < 8; ++n) TR2(lo[n], hi[n], ad[n][0], ad[n][1], (s % K1B_NST) * 16384);
    WAIT_L0();
#pragma unroll
    for (int n = 0; n < 8; ++n) {
      s16x8 b = __builtin_shufflevector(lo[n], hi[n], 0, 1, 2, 3, 4, 5, 6, 7);
#pragma unroll
      for (int m = 0; m < 2; ++m) acc[m][n] = __builtin_amdgcn_mfma_f32_16x16x32_bf16(b, a[m][s], acc[m][n], 0, 0, 0);
    }
  }
  STAMP(2);
  int tz = threadIdx.x;
  asm volatile("" : "+v"(tz));
  k1b_epilogue(acc, tz & 15, (tz & 63) >> 4, mi, j, wm, wn, wphi, kvs, o_out, h_out);
  STAMP(3);
  STAMP_OUT((unsigned long long*)(part + (4 << 20)) + 2048);
}

#define K3_NST 4
__global__ __launch_bounds__(512, 2) void k3(const short* __restrict__ x, const short* __restrict__ o_in,
                                             const short* __restrict__ h_in, const short* __restrict__ wo,
                                             const short* __restrict__ wd, short* __restrict__ out) {
  __shared__ __attribute__((aligned(16))) char smem[K3_NST * 32768];
  const int tid = threadIdx.x, lane = tid & 63, w = __builtin_amdgcn_readfirstlane(tid >> 6);
  const int bid = blockIdx.x, xcd = bid & 7, idx = bid >> 3;
  const int pm = xcd * 4 + (idx & 3), pn = idx >> 2;
  const int wm = w >> 2, wn = w & 3;
  const int lr = lane >> 3, lc = lane & 7;
  const int rr = w * 8 + lr;
  const short* ga_o = o_in + (size_t)(pm * 64 + rr) * DM + 8 * (lc ^ (rr & 7));
  const short* ga_h = h_in + (size_t)(pm * 64 + rr) * FF + 8 * (lc ^ (rr & 7));
  const short* gb_o = wo + (size_t)rr * DM + pn * 64 + 8 * (lc ^ swz128(rr));
  const short* gb_h = wd + (size_t)rr * DM + pn * 64 + 8 * (lc ^ swz128(rr));
  auto stage = [&](int i) {
    char* dst = smem + (i % K3_NST) * 32768 + w * 1024;
#pragma unroll
    for (int u = 0; u < 2; ++u) {
      int t = 2 * i + u;
      glds16((t < 8) ? (ga_o + 64 * t) : (ga_h + 64 * (t - 8)), dst + u * 16384);
      glds16((t < 8) ? (gb_o + (size_t)64 * t * DM) : (gb_h + (size_t)64 * (t - 8) * DM), dst + u * 16384 + 8192);
    }
  };
#pragma unroll
  for (int t = 0; t < K3_NST - 1; ++t) stage(t);
  f32x4 acc[2] = {};
  const int r = lane & 15, g = lane >> 4, q = r >> 2, p = r & 3;
  unsigned aad[2][2], bad[2][2];
#pragma unroll
  for (int kk = 0; kk < 2; ++kk) {
#pragma unroll
    for (int m = 0; m < 2; ++m) {
      int ra = wm * 32 + m * 16 + r;
      aad[kk][m] = lds_addr(smem) + ra * 128 + (((kk * 4 + g) ^ (ra & 7)) << 4);
    }
    int k0 = kk * 32 + 8 * g + q, k1 = k0 + 4, c = wn * 2 + (p >> 1);
    bad[kk][0] = lds_addr(smem) + 8192 + k0 * 128 + ((c ^ swz128(k0)) << 4) + 8 * (p & 1);
    bad[kk][1] = lds_addr(smem) + 8192 + k1 * 128 + ((c ^ swz128(k1)) << 4) + 8 * (p & 1);
  }
  for (int i = 0; i < 18; ++i) {
    if (i < 18 - (K3_NST - 1)) { WAIT_V(8); } else { WAIT_V(0); }
    BAR();
    const unsigned sb = (i % K3_NST) * 32768;
    s16x8 af[2][2][2];
    s16x4 lo[2][2], hi[2][2];
#pragma unroll
    for (int u = 0; u < 2; ++u)
#pragma unroll
      for (int kk = 0; kk < 2; ++kk) {
#pragma unroll
        for (int m = 0; m < 2; ++m)
          asm volatile("ds_read_b128 %0, %1 offset:%2" : "=v"(af[u][kk][m]) : "v"(aad[kk][m] + sb), "i"(u * 16384) : "memory");
        asm volatile("ds_read_b64_tr_b16 %0, %2 offset:%4\n\tds_read_b64_tr_b16 %1, %3 offset:%4"
                     : "=v"(lo[u][kk]), "=v"(hi[u][kk]) : "v"(bad[kk][0] + sb), "v"(bad[kk][1] + sb), "i"(u * 16384) : "memory");
      }
    if (i + K3_NST - 1 < 18) stage(i + K3_NST - 1);
#pragma unroll
    for (int u = 0; u < 2; ++u) {
      if (u == 0) { asm volatile("s_waitcnt lgkmcnt(8)" ::: "memory"); __builtin_amdgcn_sched_barrier(0); }
      else WAIT_L0();
#pragma unroll
      for (int kk = 0; kk < 2; ++kk) {
        s16x8 b = __builtin_shufflevector(lo[u][kk], hi[u][kk], 0, 1, 2, 3, 4, 5, 6, 7);
#pragma unroll
        for (int m = 0; m < 2; ++m) acc[m] = __builtin_amdgcn_mfma_f32_16x16x32_bf16(b, af[u][kk][m], acc[m], 0, 0, 0);
      }
    }
  }
#pragma unroll
  for (int m = 0; m < 2; ++m) {
    size_t off = (size_t)(pm * 64 + wm * 32 + m * 16 + r) * DM + pn * 64 + wn * 16 + 4 * g;
    s16x4 xv = *(const s16x4*)(x + off);
    s16x4 ov;
#pragma unroll
    for (int t = 0; t < 4; ++t) ov[t] = f2b(b2f(xv[t]) + acc[m][t]);
    *(s16x4*)(out + off) = ov;
  }
}

extern "C" void kernel_launch(void* const* d_in, const int* in_sizes, int n_in, void* d_out, int out_size,
                              void* d_ws, size_t ws_size, hipStream_t stream) {
  const short* x = (const short*)d_in[0];
  const short* wqkv = (const short*)d_in[1];
  const short* wphi = (const short*)d_in[2];
  const short* wo = (const short*)d_in[3];
  const short* wg = (const short*)d_in[5];
  const short* wu = (const short*)d_in[6];
  const short* wd = (const short*)d_in[7];
  char* ws = (char*)d_ws;
  float* part = (float*)ws;
  short* o_buf = (short*)(ws + (2 << 20));
  short* h_buf = (short*)(ws + (4 << 20));
  short* xn_buf = (short*)(ws + (12 << 20));
  k1a<<<256, 512, 0, stream>>>(x, wqkv, wphi, part, xn_buf);
  k1b<<<256, 512, 0, stream>>>(xn_buf, wqkv, wphi, wg, wu, part, o_buf, h_buf);
  k3<<<256, 512, 0, stream>>>(x, o_buf, h_buf, wo, wd, (short*)d_out);
}
```

```cpp
#include <hip/hip_runtime.h>
#include <hip/hip_bf16.h>
#include <stdint.h>

typedef __attribute__((ext_vector_type(8))) short s16x8;
typedef __attribute__((ext_vector_type(4))) short s16x4;
typedef __attribute__((ext_vector_type(4))) float f32x4;
typedef __attribute__((ext_vector_type(2))) float f32x2;
typedef __attribute__((ext_vector_type(4))) unsigned u32x4;

#define S_LEN 2048
#define DM 512
#define FF 1792
#define NH 32
#define KVP 544

#define WAIT_V(n) asm volatile("s_waitcnt vmcnt(" #n ")" ::: "memory")
#define BAR() do { asm volatile("" ::: "memory"); __builtin_amdgcn_s_barrier(); asm volatile("" ::: "memory"); } while (0)

#define STAMP(i)
#define STAMP_DECL
#define STAMP_OUT(dbg)

__device__ __forceinline__ float b2f(short v) { return __uint_as_float(((unsigned)(unsigned short)v) << 16); }
__device__ __forceinline__ short f2b(float f) {
  __bf16 h = (__bf16)f;
  return __builtin_bit_cast(short, h);
}
__device__ __forceinline__ float rb(float f) { return b2f(f2b(f)); }

__device__ __forceinline__ void glds16(const void* g, void* l) {
  __builtin_amdgcn_global_load_lds((const unsigned*)g, (unsigned*)l, 16, 0, 0);
}
__device__ __forceinline__ unsigned lds_addr(const void* p) { return (unsigned)(size_t)p; }
#define TR2(lo, hi, a0, a1, off) \
  asm volatile("ds_read_b64_tr_b16 %0, %2 offset:%4\n\tds_read_b64_tr_b16 %1, %3 offset:%4" \
               : "=&v"(lo), "=&v"(hi) : "v"(a0), "v"(a1), "i"(off) : "memory")
#define WAIT_L0() do { asm volatile("s_waitcnt lgkmcnt(0)" ::: "memory"); __builtin_amdgcn_sched_barrier(0); } while (0)

__device__ __forceinline__ int swz128(int k) { return (((k >> 1) & 1) << 1) | (((k >> 3) & 1) << 2); }
__device__ __forceinline__ int swz256(int k) { return ((k & 3) | (((k >> 3) & 1) << 2)) << 1; }

__device__ __forceinline__ f32x2 unpack2(unsigned u) {
  f32x2 r;
  r[0] = __uint_as_float(u << 16);
  r[1] = __uint_as_float(u & 0xffff0000u);
  return r;
}

__device__ __forceinline__ void sw16(float v, float& a, float& b) {
  auto r = __builtin_amdgcn_permlane16_swap(__float_as_uint(v), __float_as_uint(v), false, false);
  a = __uint_as_float(r[0]); b = __uint_as_float(r[1]);
}
__device__ __forceinline__ void sw32(float v, float& a, float& b) {
  auto r = __builtin_amdgcn_permlane32_swap(__float_as_uint(v), __float_as_uint(v), false, false);
  a = __uint_as_float(r[0]); b = __uint_as_float(r[1]);
}
__device__ __forceinline__ float red_sum(float v) { float a, b; sw16(v, a, b); v = a + b; sw32(v, a, b); return a + b; }
__device__ __forceinline__ float red_max(float v) { float a, b; sw16(v, a, b); v = fmaxf(a, b); sw32(v, a, b); return fmaxf(a, b); }
__device__ __forceinline__ float red_min(float v) { float a, b; sw16(v, a, b); v = fminf(a, b); sw32(v, a, b); return fminf(a, b); }

__device__ __forceinline__ void ln_frag(s16x8 (&a)[16]) {
  f32x2 sum2 = {0.f, 0.f};
#pragma unroll
  for (int s = 0; s < 16; ++s) {
    u32x4 u = __builtin_bit_cast(u32x4, a[s]);
#pragma unroll
    for (int j = 0; j < 4; ++j) sum2 += unpack2(u[j]);
  }
  float sum = sum2[0] + sum2[1];
  sum = red_sum(sum);
  const float mean = sum * (1.0f / DM);
  const f32x2 mean2 = {mean, mean};
  f32x2 sq2 = {0.f, 0.f};
#pragma unroll
  for (int s = 0; s < 16; ++s) {
    u32x4 u = __builtin_bit_cast(u32x4, a[s]);
#pragma unroll
    for (int j = 0; j < 4; ++j) {
      f32x2 d = unpack2(u[j]) - mean2;
      sq2 += d * d;
    }
  }
  float sq = sq2[0] + sq2[1];
  sq = red_sum(sq);
  const float rs = 1.0f / rb(sqrtf(rb(rb(sq * (1.0f / DM)) + 1e-5f)));
  const f32x2 rs2 = {rs, rs};
  const float nb = -rb(mean) * rs;
  const f32x2 nb2 = {nb, nb};
#pragma unroll
  for (int s = 0; s < 16; ++s) {
    u32x4 u = __builtin_bit_cast(u32x4, a[s]);
#pragma unroll
    for (int j = 0; j < 4; ++j) {
      f32x2 v = unpack2(u[j]) * rs2 + nb2;
      a[s][2 * j] = f2b(v[0]);
      a[s][2 * j + 1] = f2b(v[1]);
    }
  }
}

__device__ __forceinline__ void dual_softmax(const f32x4& p, s16x4& php, s16x4& phn) {
  float v[4], mx = -3.0e38f, mn = 3.0e38f;
#pragma unroll
  for (int t = 0; t < 4; ++t) { v[t] = rb(p[t]); mx = fmaxf(mx, v[t]); mn = fminf(mn, v[t]); }
  mx = red_max(mx); mn = red_min(mn);
  float ep[4], en[4], sp = 0.f, sn = 0.f;
#pragma unroll
  for (int t = 0; t < 4; ++t) { ep[t] = __expf(v[t] - mx); en[t] = __expf(mn - v[t]); sp += ep[t]; sn += en[t]; }
  sp = red_sum(sp); sn = red_sum(sn);
  const float rp = 1.0f / sp, rn = 1.0f / sn;
#pragma unroll
  for (int t = 0; t < 4; ++t) { php[t] = f2b(ep[t] * rp); phn[t] = f2b(en[t] * rn); }
}

__global__ __launch_bounds__(512, 2) void k1a(const short* __restrict__ x, const short* __restrict__ wqkv,
                                              const short* __restrict__ wphi, float* __restrict__ part,
                                              short* __restrict__ xn) {
  __shared__ __attribute__((aligned(16))) char smem[65536];
  STAMP_DECL;
  STAMP(0);
  const int tid = threadIdx.x, lane = tid & 63, w = __builtin_amdgcn_readfirstlane(tid >> 6);
  const int bid = blockIdx.x, xcd = bid & 7, idx = bid >> 3;
  const int mi = (xcd & 3) * 4 + (idx & 3), j = (xcd >> 2) * 8 + (idx >> 2);
  const int r = lane & 15, g = lane >> 4, q = r >> 2, p = r & 3;
  s16x8 a[16];
  {
    const short* px = x + (size_t)(mi * 128 + w * 16 + r) * DM + 8 * g;
#pragma unroll
    for (int s = 0; s < 16; ++s) a[s] = *(const s16x8*)(px + 32 * s);
  }
#pragma unroll
  for (int i = 0; i < 8; ++i) {
    int k = (w * 8 + i) * 8 + (lane >> 3);
    int c = (lane & 7) ^ swz128(k);
    int col = (c < 4) ? (512 + 32 * j + 8 * c) : (1024 + 32 * j + 8 * (c - 4));
    glds16(wqkv + (size_t)k * 1536 + col, smem + (w * 8 + i) * 1024);
  }
  ln_frag(a);
  if (w == (j >> 1) && (r >> 3) == (j & 1)) {
    short* pn = xn + (size_t)(mi * 128 + w * 16 + r) * DM + 8 * g;
#pragma unroll
    for (int s = 0; s < 16; ++s) *(s16x8*)(pn + 32 * s) = a[s];
  }
  STAMP(1);
  WAIT_V(0);
  __syncthreads();
  STAMP(2);
  f32x4 acc[4] = {};
  {
    unsigned ad[4][2];
#pragma unroll
    for (int n = 0; n < 4; ++n) {
      int k0 = 8 * g + q, k1 = k0 + 4, c = 2 * n + (p >> 1);
      ad[n][0] = lds_addr(smem) + k0 * 128 + ((c ^ swz128(k0)) << 4) + 8 * (p & 1);
      ad[n][1] = lds_addr(smem) + k1 * 128 + ((c ^ swz128(k1)) << 4) + 8 * (p & 1);
    }
#pragma unroll
    for (int s = 0; s < 16; ++s) {
      s16x4 lo[4], hi[4];
#pragma unroll
      for (int n = 0; n < 4; ++n) TR2(lo[n], hi[n], ad[n][0], ad[n][1], s * 4096);
      WAIT_L0();
#pragma unroll
      for (int n = 0; n < 4; ++n) {
        s16x8 b = __builtin_shufflevector(lo[n], hi[n], 0, 1, 2, 3, 4, 5, 6, 7);
        if (n < 2) acc[n] = __builtin_amdgcn_mfma_f32_16x16x32_bf16(b, a[s], acc[n], 0, 0, 0);
        else acc[n] = __builtin_amdgcn_mfma_f32_16x16x32_bf16(a[s], b, acc[n], 0, 0, 0);
      }
    }
  }
  STAMP(3);
  __syncthreads();
  float* red = (float*)smem;
  s16x4 ident;
#pragma unroll
  for (int t = 0; t < 4; ++t) ident[t] = (4 * g + t == r) ? (short)0x3f80 : (short)0;
#pragma unroll
  for (int hh = 0; hh < 2; ++hh) {
    const int head = 2 * j + hh;
    s16x4 kb, wa, va;
#pragma unroll
    for (int t = 0; t < 4; ++t) {
      kb[t] = f2b(acc[hh][t]);
      va[t] = f2b(acc[2 + hh][t]);
      wa[t] = wphi[head * 256 + (4 * g + t) * 16 + r];
    }
    f32x4 pk = {};
    pk = __builtin_amdgcn_mfma_f32_16x16x16bf16_1k(wa, kb, pk, 0, 0, 0);
    s16x4 php, phn;
    dual_softmax(pk, php, phn);
    f32x4 tp = {}, tn = {};
    tp = __builtin_amdgcn_mfma_f32_16x16x16bf16_1k(php, ident, tp, 0, 0, 0);
    tn = __builtin_amdgcn_mfma_f32_16x16x16bf16_1k(phn, ident, tn, 0, 0, 0);
    s16x4 bp, bn;
    float zp = 0.f, zn = 0.f;
#pragma unroll
    for (int t = 0; t < 4; ++t) { bp[t] = f2b(tp[t]); bn[t] = f2b(tn[t]); zp += tp[t]; zn += tn[t]; }
    zp = red_sum(zp); zn = red_sum(zn);
    f32x4 kp = {}, kn = {};
    kp = __builtin_amdgcn_mfma_f32_16x16x16bf16_1k(va, bp, kp, 0, 0, 0);
    kn = __builtin_amdgcn_mfma_f32_16x16x16bf16_1k(va, bn, kn, 0, 0, 0);
    float* rr = red + (w * 2 + hh) * KVP;
    *(f32x4*)(rr + r * 16 + 4 * g) = kp;
    *(f32x4*)(rr + (16 + r) * 16 + 4 * g) = kn;
    if (g == 0) {
      rr[512 + r] = zp;
      rr[512 + 16 + r] = zn;
    }
  }
  __syncthreads();
  for (int e = tid; e < 2 * KVP; e += 512) {
    float s = 0.f;
#pragma unroll
    for (int ww = 0; ww < 8; ++ww) s += red[ww * 2 * KVP + e];
    part[((size_t)mi * NH + 2 * j) * KVP + e] = s;
  }
  STAMP(4);
  STAMP_OUT((unsigned long long*)(part + (4 << 20)));
}

__device__ __forceinline__ void k1b_epilogue(f32x4 (&acc)[2][8], int r, int g, int mi, int j, int wm, int wn,
                                             const short* wphi, const float* kvs,
                                             short* __restrict__ o_out, short* __restrict__ h_out) {
#pragma unroll
  for (int m = 0; m < 2; ++m) {
    const size_t row = (size_t)mi * 128 + wm * 32 + m * 16 + r;
    if (wn == 0) {
#pragma unroll
      for (int hh = 0; hh < 2; ++hh) {
        const int head = 2 * j + hh;
        const float* kv = kvs + hh * KVP;
        s16x4 qb, wa, kap, kan;
#pragma unroll
        for (int t = 0; t < 4; ++t) {
          qb[t] = f2b(acc[m][hh][t]);
          wa[t] = wphi[hh * 256 + (4 * g + t) * 16 + r];
          kap[t] = f2b(kv[(4 * g + t) * 16 + r]);
          kan[t] = f2b(kv[(16 + 4 * g + t) * 16 + r]);
        }
        f32x4 pq = {};
        pq = __builtin_amdgcn_mfma_f32_16x16x16bf16_1k(wa, qb, pq, 0, 0, 0);
        s16x4 php, phn;
        dual_softmax(pq, php, phn);
        float den = 0.f;
#pragma unroll
        for (int t = 0; t < 4; ++t)
          den += b2f(php[t]) * kv[512 + 4 * g + t] + b2f(phn[t]) * kv[512 + 16 + 4 * g + t];
        den = red_sum(den);
        const float rd = 1.0f / rb(rb(den) + 1e-6f);
        f32x4 num = {};
        num = __builtin_amdgcn_mfma_f32_16x16x16bf16_1k(kap, php, num, 0, 0, 0);
        num = __builtin_amdgcn_mfma_f32_16x16x16bf16_1k(kan, phn, num, 0, 0, 0);
        s16x4 ov;
#pragma unroll
        for (int t = 0; t < 4; ++t) ov[t] = f2b(rb(num[t]) * rd);
        *(s16x4*)(o_out + row * DM + head * 16 + 4 * g) = ov;
      }
#pragma unroll
      for (int n = 0; n < 3; ++n) {
        s16x4 hv;
#pragma unroll
        for (int t = 0; t < 4; ++t) hv[t] = f2b(fmaxf(rb(acc[m][2 + n][t]), 0.f) * rb(acc[m][5 + n][t]));
        *(s16x4*)(h_out + row * FF + 112 * j + 16 * n + 4 * g) = hv;
      }
    } else {
#pragma unroll
      for (int n = 0; n < 4; ++n) {
        s16x4 hv;
#pragma unroll
        for (int t = 0; t < 4; ++t) hv[t] = f2b(fmaxf(rb(acc[m][n][t]), 0.f) * rb(acc[m][4 + n][t]));
        *(s16x4*)(h_out + row * FF + 112 * j + 48 + 16 * n + 4 * g) = hv;
      }
    }
  }
}

__global__ __launch_bounds__(512, 2) void k1b(const short* __restrict__ xn, const short* __restrict__ wqkv,
                                              const short* __restrict__ wphi, const short* __restrict__ wg,
                                              const short* __restrict__ wu, const float* __restrict__ part,
                                              short* __restrict__ o_out, short* __restrict__ h_out) {
  __shared__ __attribute__((aligned(16))) char smem[131072];
  STAMP_DECL;
  STAMP(0);
  const int tid = threadIdx.x, lane = tid & 63, w = __builtin_amdgcn_readfirstlane(tid >> 6);
  const int bid = blockIdx.x, xcd = bid & 7, idx = bid >> 3;
  const int mi = (xcd & 3) * 4 + (idx & 3), j = (xcd >> 2) * 8 + (idx >> 2);
  const int wm = w >> 1, wn = w & 1;
  const int r = lane & 15, g = lane >> 4, q = r >> 2, p = r & 3;
#pragma unroll
  for (int i = 0; i < 16; ++i)
    glds16(xn + (size_t)(mi * 128 + w * 16 + i) * DM + 8 * (lane ^ i), smem + (w * 16 + i) * 1024);
  const short* src[2];
  int sstride[2];
#pragma unroll
  for (int i = 0; i < 2; ++i) {
    int kr = (w * 2 + i) * 2 + (lane >> 5);
    int c = (lane & 31) ^ swz256(kr);
    const short* b; int st;
    if (c < 4) { b = wqkv + 32 * j + 8 * c; st = 1536; }
    else if (c < 10) { b = wg + 112 * j + 8 * (c - 4); st = FF; }
    else if (c < 16) { b = wu + 112 * j + 8 * (c - 10); st = FF; }
    else if (c < 24) { b = wg + 112 * j + 48 + 8 * (c - 16); st = FF; }
    else { b = wu + 112 * j + 48 + 8 * (c - 24); st = FF; }
    src[i] = b + (size_t)kr * st;
    sstride[i] = 32 * st;
  }
#define K1B_SLOT(t) (((t) & 7) * 16384)
  auto stage = [&](int t) {
    char* dst = smem + K1B_SLOT(t) + (w * 2) * 1024;
    glds16(src[0] + (size_t)t * sstride[0], dst);
    glds16(src[1] + (size_t)t * sstride[1], dst + 1024);
  };
  float kvr[3];
#pragma unroll
  for (int u = 0; u < 3; ++u) {
    int e = tid + u * 512;
    float s = 0.f;
    if (e < 2 * KVP) {
#pragma unroll
      for (int i = 0; i < 16; ++i) s += part[((size_t)i * NH + 2 * j) * KVP + e];
    }
    kvr[u] = rb(s);
  }
  WAIT_V(0);
  BAR();
  s16x8 a[2][16];
#pragma unroll
  for (int t = 0; t < 2; ++t)
#pragma unroll
    for (int s = 0; s < 16; ++s)
      asm volatile("ds_read_b128 %0, %1" : "=v"(a[t][s])
                   : "v"(lds_addr(smem) + (wm * 32 + t * 16 + r) * 1024 + (((4 * s + g) ^ r) << 4)) : "memory");
  WAIT_L0();
  BAR();
  STAMP(1);
#pragma unroll
  for (int t = 0; t < 6; ++t) stage(t);
  f32x4 acc[2][8] = {};
  unsigned ad[8][2];
#pragma unroll
  for (int n = 0; n < 8; ++n) {
    int k0 = 8 * g + q, k1 = k0 + 4, c = wn * 16 + 2 * n + (p >> 1);
    ad[n][0] = lds_addr(smem) + k0 * 512 + ((c ^ swz256(k0)) << 4) + 8 * (p & 1);
    ad[n][1] = lds_addr(smem) + k1 * 512 + ((c ^ swz256(k1)) << 4) + 8 * (p & 1);
  }
  s16x4 lo[2][4], hi[2][4];
#define K1B_READ(s_, h_) do { \
    unsigned sbase = K1B_SLOT(s_) & ~65535; \
    asm volatile("" : "+s"(sbase)); \
    _Pragma("unroll") for (int n = 0; n < 4; ++n) \
      TR2(lo[h_][n], hi[h_][n], ad[4 * (h_) + n][0] + sbase, ad[4 * (h_) + n][1] + sbase, K1B_SLOT(s_) & 65535); \
  } while (0)
#define K1B_MMA(s_, h_) do { \
    _Pragma("unroll") for (int n = 0; n < 4; ++n) { \
      s16x8 b = __builtin_shufflevector(lo[h_][n], hi[h_][n], 0, 1, 2, 3, 4, 5, 6, 7); \
      _Pragma("unroll") for (int m = 0; m < 2; ++m) \
        acc[m][4 * (h_) + n] = __builtin_amdgcn_mfma_f32_16x16x32_bf16(b, a[m][s_], acc[m][4 * (h_) + n], 0, 0, 0); \
    } \
  } while (0)
#define WAIT_L8() do { asm volatile("s_waitcnt lgkmcnt(8)" ::: "memory"); __builtin_amdgcn_sched_barrier(0); } while (0)
#pragma unroll
  for (int s = 0; s < 16; ++s) {
    if (s <= 10) { WAIT_V(10); } else if (s == 11) { WAIT_V(8); } else if (s == 12) { WAIT_V(6); }
    else if (s == 13) { WAIT_V(4); } else if (s == 14) { WAIT_V(2); } else { WAIT_V(0); }
    BAR();
    K1B_READ(s, 0);
    if (s + 6 < 16) stage(s + 6);
    if (s > 0) { WAIT_L8(); K1B_MMA(s - 1, 1); }
    K1B_READ(s, 1);
    WAIT_L8();
    K1B_MMA(s, 0);
  }
  WAIT_L0();
  K1B_MMA(15, 1);
  STAMP(2);
  float* kvs = (float*)smem;
  __syncthreads();
#pragma unroll
  for (int u = 0; u < 3; ++u) if (tid + u * 512 < 2 * KVP) kvs[tid + u * 512] = kvr[u];
  __syncthreads();
  int tz = threadIdx.x;
  asm volatile("" : "+v"(tz));
  k1b_epilogue(acc, tz & 15, (tz & 63) >> 4, mi, j, wm, wn, wphi + j * 512, kvs, o_out, h_out);
  STAMP(3);
  STAMP_OUT((unsigned long long*)(part + (4 << 20)) + 2048);
}

#define K3_NST 6
#define K3_D 5
__global__ __launch_bounds__(512, 2) void k3(const short* __restrict__ x, const short* __restrict__ o_in,
                                             const short* __restrict__ h_in, const short* __restrict__ wo,
                                             const short* __restrict__ wd, short* __restrict__ out) {
  __shared__ __attribute__((aligned(16))) char smem[K3_NST * 16384];
  const int tid = threadIdx.x, lane = tid & 63, w = __builtin_amdgcn_readfirstlane(tid >> 6);
  const int bid = blockIdx.x, xcd = bid & 7, idx = bid >> 3;
  const int pm = xcd * 4 + (idx & 3), pn = idx >> 2;
  const int kw = w >> 2, wm = w & 3;
  const int lr = lane >> 3, lc = lane & 7;
  const int r = lane & 15, g = lane >> 4, q = r >> 2, p = r & 3;
  const int rr = w * 8 + lr;
  const short* ga_o = o_in + (size_t)(pm * 64 + wm * 16 + r) * DM + 8 * g + 64 * kw;
  const short* ga_h = h_in + (size_t)(pm * 64 + wm * 16 + r) * FF + 8 * g + 64 * kw - 512;
  const short* gb_o = wo + (size_t)rr * DM + pn * 64 + 8 * (lc ^ swz128(rr));
  const short* gb_h = wd + ((size_t)rr - 512) * DM + pn * 64 + 8 * (lc ^ swz128(rr));
  s16x8 af[K3_D + 1][2];
  auto loada = [&](int i, s16x8 (&dst)[2]) {
    const short* pa = (i < 4) ? (ga_o + 128 * i) : (ga_h + 128 * i);
    asm volatile("global_load_dwordx4 %0, %2, off\n\tglobal_load_dwordx4 %1, %2, off offset:64"
                 : "=&v"(dst[0]), "=&v"(dst[1]) : "v"(pa) : "memory");
  };
  auto stage = [&](int i) {
    char* dst = smem + (i % K3_NST) * 16384 + w * 1024;
    const short* b0 = (i < 4) ? gb_o : gb_h;
    glds16(b0 + (size_t)128 * i * DM, dst);
    glds16(b0 + (size_t)(128 * i + 64) * DM, dst + 8192);
  };
#pragma unroll
  for (int i = 0; i < K3_D; ++i) { loada(i, af[i]); stage(i); }
  f32x4 acc[4] = {};
  unsigned bad[2][4][2];
#pragma unroll
  for (int kk = 0; kk < 2; ++kk)
#pragma unroll
    for (int n = 0; n < 4; ++n) {
      int k0 = kk * 32 + 8 * g + q, k1 = k0 + 4, c = n * 2 + (p >> 1);
      bad[kk][n][0] = lds_addr(smem) + kw * 8192 + k0 * 128 + ((c ^ swz128(k0)) << 4) + 8 * (p & 1);
      bad[kk][n][1] = lds_addr(smem) + kw * 8192 + k1 * 128 + ((c ^ swz128(k1)) << 4) + 8 * (p & 1);
    }
#pragma unroll 6
  for (int i = 0; i < 18; ++i) {
    if (i < 18 - (K3_D - 1)) { WAIT_V(16); } else { WAIT_V(0); }
    BAR();
    unsigned sb = (i % K3_NST) * 16384;
    s16x4 lo[2][4], hi[2][4];
#pragma unroll
    for (int kk = 0; kk < 2; ++kk)
#pragma unroll
      for (int n = 0; n < 4; ++n)
        asm volatile("ds_read_b64_tr_b16 %0, %2\n\tds_read_b64_tr_b16 %1, %3"
                     : "=&v"(lo[kk][n]), "=&v"(hi[kk][n]) : "v"(bad[kk][n][0] + sb), "v"(bad[kk][n][1] + sb) : "memory");
    if (i + K3_D < 18) { loada(i + K3_D, af[(i + K3_D) % (K3_D + 1)]); stage(i + K3_D); }
#pragma unroll
    for (int kk = 0; kk < 2; ++kk) {
      if (kk == 0) { asm volatile("s_waitcnt lgkmcnt(8)" ::: "memory"); __builtin_amdgcn_sched_barrier(0); }
      else WAIT_L0();
#pragma unroll
      for (int n = 0; n < 4; ++n) {
        s16x8 b = __builtin_shufflevector(lo[kk][n], hi[kk][n], 0, 1, 2, 3, 4, 5, 6, 7);
        acc[n] = __builtin_amdgcn_mfma_f32_16x16x32_bf16(b, af[i % (K3_D + 1)][kk], acc[n], 0, 0, 0);
      }
    }
  }
  __syncthreads();
  f32x4* red = (f32x4*)smem;
  if (kw == 1) {
#pragma unroll
    for (int n = 0; n < 4; ++n) red[(wm * 4 + n) * 64 + lane] = acc[n];
  }
  __syncthreads();
  if (kw == 0) {
#pragma unroll
    for (int n = 0; n < 4; ++n) {
      f32x4 o2 = red[(wm * 4 + n) * 64 + lane];
      size_t off = (size_t)(pm * 64 + wm * 16 + r) * DM + pn * 64 + n * 16 + 4 * g;
      s16x4 xv = *(const s16x4*)(x + off);
      s16x4 ov;
#pragma unroll
      for (int t = 0; t < 4; ++t) ov[t] = f2b(b2f(xv[t]) + (acc[n][t] + o2[t]));
      *(s16x4*)(out + off) = ov;
    }
  }
}

extern "C" void kernel_launch(void* const* d_in, const int* in_sizes, int n_in, void* d_out, int out_size,
                              void* d_ws, size_t ws_size, hipStream_t stream) {
  const short* x = (const short*)d_in[0];
  const short* wqkv = (const short*)d_in[1];
  const short* wphi = (const short*)d_in[2];
  const short* wo = (const short*)d_in[3];
  const short* wg = (const short*)d_in[5];
  const short* wu = (const short*)d_in[6];
  const short* wd = (const short*)d_in[7];
  char* ws = (char*)d_ws;
  float* part = (float*)ws;
  short* o_buf = (short*)(ws + (2 << 20));
  short* h_buf = (short*)(ws + (4 << 20));
  short* xn_buf = (short*)(ws + (12 << 20));
  k1a<<<256, 512, 0, stream>>>(x, wqkv, wphi, part, xn_buf);
  k1b<<<256, 512, 0, stream>>>(xn_buf, wqkv, wphi, wg, wu, part, o_buf, h_buf);
  k3<<<256, 512, 0, stream>>>(x, o_buf, h_buf, wo, wd, (short*)d_out);
}
```

```cpp
#include <hip/hip_runtime.h>
#include <hip/hip_bf16.h>
#include <stdint.h>

typedef __attribute__((ext_vector_type(8))) short s16x8;
typedef __attribute__((ext_vector_type(4))) short s16x4;
typedef __attribute__((ext_vector_type(4))) float f32x4;
typedef __attribute__((ext_vector_type(2))) float f32x2;
typedef __attribute__((ext_vector_type(4))) unsigned u32x4;

#define S_LEN 2048
#define DM 512
#define FF 1792
#define NH 32
#define KVP 544

#define WAIT_V(n) asm volatile("s_waitcnt vmcnt(" #n ")" ::: "memory")
#define BAR() do { asm volatile("" ::: "memory"); __builtin_amdgcn_s_barrier(); asm volatile("" ::: "memory"); } while (0)

#define STAMP(i)
#define STAMP_DECL
#define STAMP_OUT(dbg)

__device__ __forceinline__ float b2f(short v) { return __uint_as_float(((unsigned)(unsigned short)v) << 16); }
__device__ __forceinline__ short f2b(float f) {
  __bf16 h = (__bf16)f;
  return __builtin_bit_cast(short, h);
}
__device__ __forceinline__ float rb(float f) { return b2f(f2b(f)); }

__device__ __forceinline__ void glds16(const void* g, void* l) {
  __builtin_amdgcn_global_load_lds((const unsigned*)g, (unsigned*)l, 16, 0, 0);
}
__device__ __forceinline__ unsigned lds_addr(const void* p) { return (unsigned)(size_t)p; }
#define TR2(lo, hi, a0, a1, off) \
  asm volatile("ds_read_b64_tr_b16 %0, %2 offset:%4\n\tds_read_b64_tr_b16 %1, %3 offset:%4" \
               : "=&v"(lo), "=&v"(hi) : "v"(a0), "v"(a1), "i"(off) : "memory")
#define WAIT_L0() do { asm volatile("s_waitcnt lgkmcnt(0)" ::: "memory"); __builtin_amdgcn_sched_barrier(0); } while (0)

__device__ __forceinline__ int swz128(int k) { return (((k >> 1) & 1) << 1) | (((k >> 3) & 1) << 2); }
__device__ __forceinline__ int swz256(int k) { return ((k & 3) | (((k >> 3) & 1) << 2)) << 1; }

__device__ __forceinline__ f32x2 unpack2(unsigned u) {
  f32x2 r;
  r[0] = __uint_as_float(u << 16);
  r[1] = __uint_as_float(u & 0xffff0000u);
  return r;
}

__device__ __forceinline__ void sw16(float v, float& a, float& b) {
  auto r = __builtin_amdgcn_permlane16_swap(__float_as_uint(v), __float_as_uint(v), false, false);
  a = __uint_as_float(r[0]); b = __uint_as_float(r[1]);
}
__device__ __forceinline__ void sw32(float v, float& a, float& b) {
  auto r = __builtin_amdgcn_permlane32_swap(__float_as_uint(v), __float_as_uint(v), false, false);
  a = __uint_as_float(r[0]); b = __uint_as_float(r[1]);
}
__device__ __forceinline__ float red_sum(float v) { float a, b; sw16(v, a, b); v = a + b; sw32(v, a, b); return a + b; }
__device__ __forceinline__ float red_max(float v) { float a, b; sw16(v, a, b); v = fmaxf(a, b); sw32(v, a, b); return fmaxf(a, b); }
__device__ __forceinline__ float red_min(float v) { float a, b; sw16(v, a, b); v = fminf(a, b); sw32(v, a, b); return fminf(a, b); }

__device__ __forceinline__ void ln_frag(s16x8 (&a)[16]) {
  f32x2 sum2 = {0.f, 0.f};
#pragma unroll
  for (int s = 0; s < 16; ++s) {
    u32x4 u = __builtin_bit_cast(u32x4, a[s]);
#pragma unroll
    for (int j = 0; j < 4; ++j) sum2 += unpack2(u[j]);
  }
  float sum = sum2[0] + sum2[1];
  sum = red_sum(sum);
  const float mean = sum * (1.0f / DM);
  const f32x2 mean2 = {mean, mean};
  f32x2 sq2 = {0.f, 0.f};
#pragma unroll
  for (int s = 0; s < 16; ++s) {
    u32x4 u = __builtin_bit_cast(u32x4, a[s]);
#pragma unroll
    for (int j = 0; j < 4; ++j) {
      f32x2 d = unpack2(u[j]) - mean2;
      sq2 += d * d;
    }
  }
  float sq = sq2[0] + sq2[1];
  sq = red_sum(sq);
  const float rs = 1.0f / rb(sqrtf(rb(rb(sq * (1.0f / DM)) + 1e-5f)));
  const f32x2 rs2 = {rs, rs};
  const float nb = -rb(mean) * rs;
  const f32x2 nb2 = {nb, nb};
#pragma unroll
  for (int s = 0; s < 16; ++s) {
    u32x4 u = __builtin_bit_cast(u32x4, a[s]);
#pragma unroll
    for (int j = 0; j < 4; ++j) {
      f32x2 v = unpack2(u[j]) * rs2 + nb2;
      a[s][2 * j] = f2b(v[0]);
      a[s][2 * j + 1] = f2b(v[1]);
    }
  }
}

__device__ __forceinline__ void dual_softmax(const f32x4& p, s16x4& php, s16x4& phn) {
  float v[4], mx = -3.0e38f, mn = 3.0e38f;
#pragma unroll
  for (int t = 0; t < 4; ++t) { v[t] = rb(p[t]); mx = fmaxf(mx, v[t]); mn = fminf(mn, v[t]); }
  mx = red_max(mx); mn = red_min(mn);
  float ep[4], en[4], sp = 0.f, sn = 0.f;
#pragma unroll
  for (int t = 0; t < 4; ++t) { ep[t] = __expf(v[t] - mx); en[t] = __expf(mn - v[t]); sp += ep[t]; sn += en[t]; }
  sp = red_sum(sp); sn = red_sum(sn);
  const float rp = 1.0f / sp, rn = 1.0f / sn;
#pragma unroll
  for (int t = 0; t < 4; ++t) { php[t] = f2b(ep[t] * rp); phn[t] = f2b(en[t] * rn); }
}

__global__ __launch_bounds__(512, 2) void k1a(const short* __restrict__ x, const short* __restrict__ wqkv,
                                              const short* __restrict__ wphi, float* __restrict__ part,
                                              short* __restrict__ xn) {
  __shared__ __attribute__((aligned(16))) char smem[65536];
  STAMP_DECL;
  STAMP(0);
  const int tid = threadIdx.x, lane = tid & 63, w = __builtin_amdgcn_readfirstlane(tid >> 6);
  const int bid = blockIdx.x, xcd = bid & 7, idx = bid >> 3;
  const int mi = (xcd & 3) * 4 + (idx & 3), j = (xcd >> 2) * 8 + (idx >> 2);
  const int r = lane & 15, g = lane >> 4, q = r >> 2, p = r & 3;
  s16x8 a[16];
  {
    const short* px = x + (size_t)(mi * 128 + w * 16 + r) * DM + 8 * g;
#pragma unroll
    for (int s = 0; s < 16; ++s) a[s] = *(const s16x8*)(px + 32 * s);
  }
#pragma unroll
  for (int i = 0; i < 8; ++i) {
    int k = (w * 8 + i) * 8 + (lane >> 3);
    int c = (lane & 7) ^ swz128(k);
    int col = (c < 4) ? (512 + 32 * j + 8 * c) : (1024 + 32 * j + 8 * (c - 4));
    glds16(wqkv + (size_t)k * 1536 + col, smem + (w * 8 + i) * 1024);
  }
  ln_frag(a);
  if (w == (j >> 1) && (r >> 3) == (j & 1)) {
    short* pn = xn + (size_t)(mi * 128 + w * 16 + r) * DM + 8 * g;
#pragma unroll
    for (int s = 0; s < 16; ++s) *(s16x8*)(pn + 32 * s) = a[s];
  }
  STAMP(1);
  WAIT_V(0);
  __syncthreads();
  STAMP(2);
  f32x4 acc[4] = {};
  {
    unsigned ad[4][2];
#pragma unroll
    for (int n = 0; n < 4; ++n) {
      int k0 = 8 * g + q, k1 = k0 + 4, c = 2 * n + (p >> 1);
      ad[n][0] = lds_addr(smem) + k0 * 128 + ((c ^ swz128(k0)) << 4) + 8 * (p & 1);
      ad[n][1] = lds_addr(smem) + k1 * 128 + ((c ^ swz128(k1)) << 4) + 8 * (p & 1);
    }
#pragma unroll
    for (int s = 0; s < 16; ++s) {
      s16x4 lo[4], hi[4];
#pragma unroll
      for (int n = 0; n < 4; ++n) TR2(lo[n], hi[n], ad[n][0], ad[n][1], s * 4096);
      WAIT_L0();
#pragma unroll
      for (int n = 0; n < 4; ++n) {
        s16x8 b = __builtin_shufflevector(lo[n], hi[n], 0, 1, 2, 3, 4, 5, 6, 7);
        if (n < 2) acc[n] = __builtin_amdgcn_mfma_f32_16x16x32_bf16(b, a[s], acc[n], 0, 0, 0);
        else acc[n] = __builtin_amdgcn_mfma_f32_16x16x32_bf16(a[s], b, acc[n], 0, 0, 0);
      }
    }
  }
  STAMP(3);
  __syncthreads();
  float* red = (float*)smem;
  s16x4 ident;
#pragma unroll
  for (int t = 0; t < 4; ++t) ident[t] = (4 * g + t == r) ? (short)0x3f80 : (short)0;
#pragma unroll
  for (int hh = 0; hh < 2; ++hh) {
    const int head = 2 * j + hh;
    s16x4 kb, wa, va;
#pragma unroll
    for (int t = 0; t < 4; ++t) {
      kb[t] = f2b(acc[hh][t]);
      va[t] = f2b(acc[2 + hh][t]);
      wa[t] = wphi[head * 256 + (4 * g + t) * 16 + r];
    }
    f32x4 pk = {};
    pk = __builtin_amdgcn_mfma_f32_16x16x16bf16_1k(wa, kb, pk, 0, 0, 0);
    s16x4 php, phn;
    dual_softmax(pk, php, phn);
    f32x4 tp = {}, tn = {};
    tp = __builtin_amdgcn_mfma_f32_16x16x16bf16_1k(php, ident, tp, 0, 0, 0);
    tn = __builtin_amdgcn_mfma_f32_16x16x16bf16_1k(phn, ident, tn, 0, 0, 0);
    s16x4 bp, bn;
    float zp = 0.f, zn = 0.f;
#pragma unroll
    for (int t = 0; t < 4; ++t) { bp[t] = f2b(tp[t]); bn[t] = f2b(tn[t]); zp += tp[t]; zn += tn[t]; }
    zp = red_sum(zp); zn = red_sum(zn);
    f32x4 kp = {}, kn = {};
    kp = __builtin_amdgcn_mfma_f32_16x16x16bf16_1k(va, bp, kp, 0, 0, 0);
    kn = __builtin_amdgcn_mfma_f32_16x16x16bf16_1k(va, bn, kn, 0, 0, 0);
    float* rr = red + (w * 2 + hh) * KVP;
    *(f32x4*)(rr + r * 16 + 4 * g) = kp;
    *(f32x4*)(rr + (16 + r) * 16 + 4 * g) = kn;
    if (g == 0) {
      rr[512 + r] = zp;
      rr[512 + 16 + r] = zn;
    }
  }
  __syncthreads();
  for (int e = tid; e < 2 * KVP; e += 512) {
    float s = 0.f;
#pragma unroll
    for (int ww = 0; ww < 8; ++ww) s += red[ww * 2 * KVP + e];
    part[((size_t)mi * NH + 2 * j) * KVP + e] = s;
  }
  STAMP(4);
  STAMP_OUT((unsigned long long*)(part + (4 << 20)));
}

__device__ __forceinline__ void k1b_epilogue(f32x4 (&acc)[2][8], int r, int g, int mi, int j, int wm, int wn,
                                             const short* wphi, const float* kvs,
                                             short* __restrict__ o_out, short* __restrict__ h_out) {
#pragma unroll
  for (int m = 0; m < 2; ++m) {
    const size_t row = (size_t)mi * 128 + wm * 32 + m * 16 + r;
    if (wn == 0) {
#pragma unroll
      for (int hh = 0; hh < 2; ++hh) {
        const int head = 2 * j + hh;
        const float* kv = kvs + hh * KVP;
        s16x4 qb, wa, kap, kan;
#pragma unroll
        for (int t = 0; t < 4; ++t) {
          qb[t] = f2b(acc[m][hh][t]);
          wa[t] = wphi[hh * 256 + (4 * g + t) * 16 + r];
          kap[t] = f2b(kv[(4 * g + t) * 16 + r]);
          kan[t] = f2b(kv[(16 + 4 * g + t) * 16 + r]);
        }
        f32x4 pq = {};
        pq = __builtin_amdgcn_mfma_f32_16x16x16bf16_1k(wa, qb, pq, 0, 0, 0);
        s16x4 php, phn;
        dual_softmax(pq, php, phn);
        float den = 0.f;
#pragma unroll
        for (int t = 0; t < 4; ++t)
          den += b2f(php[t]) * kv[512 + 4 * g + t] + b2f(phn[t]) * kv[512 + 16 + 4 * g + t];
        den = red_sum(den);
        const float rd = 1.0f / rb(rb(den) + 1e-6f);
        f32x4 num = {};
        num = __builtin_amdgcn_mfma_f32_16x16x16bf16_1k(kap, php, num, 0, 0, 0);
        num = __builtin_amdgcn_mfma_f32_16x16x16bf16_1k(kan, phn, num, 0, 0, 0);
        s16x4 ov;
#pragma unroll
        for (int t = 0; t < 4; ++t) ov[t] = f2b(rb(num[t]) * rd);
        *(s16x4*)(o_out + row * DM + head * 16 + 4 * g) = ov;
      }
#pragma unroll
      for (int n = 0; n < 3; ++n) {
        s16x4 hv;
#pragma unroll
        for (int t = 0; t < 4; ++t) hv[t] = f2b(fmaxf(rb(acc[m][2 + n][t]), 0.f) * rb(acc[m][5 + n][t]));
        *(s16x4*)(h_out + row * FF + 112 * j + 16 * n + 4 * g) = hv;
      }
    } else {
#pragma unroll
      for (int n = 0; n < 4; ++n) {
        s16x4 hv;
#pragma unroll
        for (int t = 0; t < 4; ++t) hv[t] = f2b(fmaxf(rb(acc[m][n][t]), 0.f) * rb(acc[m][4 + n][t]));
        *(s16x4*)(h_out + row * FF + 112 * j + 48 + 16 * n + 4 * g) = hv;
      }
    }
  }
}

__global__ __launch_bounds__(512, 2) void k1b(const short* __restrict__ xn, const short* __restrict__ wqkv,
                                              const short* __restrict__ wphi, const short* __restrict__ wg,
                                              const short* __restrict__ wu, const float* __restrict__ part,
                                              short* __restrict__ o_out, short* __restrict__ h_out) {
  __shared__ __attribute__((aligned(16))) char smem[131072];
  STAMP_DECL;
  STAMP(0);
  const int tid = threadIdx.x, lane = tid & 63, w = __builtin_amdgcn_readfirstlane(tid >> 6);
  const int bid = blockIdx.x, xcd = bid & 7, idx = bid >> 3;
  const int mi = (xcd & 3) * 4 + (idx & 3), j = (xcd >> 2) * 8 + (idx >> 2);
  const int wm = w >> 1, wn = w & 1;
  const int r = lane & 15, g = lane >> 4, q = r >> 2, p = r & 3;
#pragma unroll
  for (int i = 0; i < 16; ++i)
    glds16(xn + (size_t)(mi * 128 + w * 16 + i) * DM + 8 * (lane ^ i), smem + (w * 16 + i) * 1024);
  const short* src[2];
  int sstride[2];
#pragma unroll
  for (int i = 0; i < 2; ++i) {
    int kr = (w * 2 + i) * 2 + (lane >> 5);
    int c = (lane & 31) ^ swz256(kr);
    const short* b; int st;
    if (c < 4) { b = wqkv + 32 * j + 8 * c; st = 1536; }
    else if (c < 10) { b = wg + 112 * j + 8 * (c - 4); st = FF; }
    else if (c < 16) { b = wu + 112 * j + 8 * (c - 10); st = FF; }
    else if (c < 24) { b = wg + 112 * j + 48 + 8 * (c - 16); st = FF; }
    else { b = wu + 112 * j + 48 + 8 * (c - 24); st = FF; }
    src[i] = b + (size_t)kr * st;
    sstride[i] = 32 * st;
  }
#define K1B_SLOT(t) (((t) & 7) * 16384)
  auto stage = [&](int t) {
    char* dst = smem + K1B_SLOT(t) + (w * 2) * 1024;
    glds16(src[0] + (size_t)t * sstride[0], dst);
    glds16(src[1] + (size_t)t * sstride[1], dst + 1024);
  };
  float kvr[3];
#pragma unroll
  for (int u = 0; u < 3; ++u) {
    int e = tid + u * 512;
    float s = 0.f;
    if (e < 2 * KVP) {
#pragma unroll
      for (int i = 0; i < 16; ++i) s += part[((size_t)i * NH + 2 * j) * KVP + e];
    }
    kvr[u] = rb(s);
  }
  WAIT_V(0);
  BAR();
  s16x8 a[2][16];
#pragma unroll
  for (int t = 0; t < 2; ++t)
#pragma unroll
    for (int s = 0; s < 16; ++s)
      asm volatile("ds_read_b128 %0, %1" : "=v"(a[t][s])
                   : "v"(lds_addr(smem) + (wm * 32 + t * 16 + r) * 1024 + (((4 * s + g) ^ r) << 4)) : "memory");
  WAIT_L0();
  BAR();
  STAMP(1);
#pragma unroll
  for (int t = 0; t < 6; ++t) stage(t);
  f32x4 acc[2][8] = {};
  unsigned ad[8][2];
#pragma unroll
  for (int n = 0; n < 8; ++n) {
    int k0 = 8 * g + q, k1 = k0 + 4, c = wn * 16 + 2 * n + (p >> 1);
    ad[n][0] = lds_addr(smem) + k0 * 512 + ((c ^ swz256(k0)) << 4) + 8 * (p & 1);
    ad[n][1] = lds_addr(smem) + k1 * 512 + ((c ^ swz256(k1)) << 4) + 8 * (p & 1);
  }
  s16x4 lo[2][4], hi[2][4];
#define K1B_READ(s_, h_) do { \
    unsigned sbase = K1B_SLOT(s_) & ~65535; \
    asm volatile("" : "+s"(sbase)); \
    _Pragma("unroll") for (int n = 0; n < 4; ++n) \
      TR2(lo[h_][n], hi[h_][n], ad[4 * (h_) + n][0] + sbase, ad[4 * (h_) + n][1] + sbase, K1B_SLOT(s_) & 65535); \
  } while (0)
#define K1B_MMA(s_, h_) do { \
    _Pragma("unroll") for (int n = 0; n < 4; ++n) { \
      s16x8 b = __builtin_shufflevector(lo[h_][n], hi[h_][n], 0, 1, 2, 3, 4, 5, 6, 7); \
      _Pragma("unroll") for (int m = 0; m < 2; ++m) \
        acc[m][4 * (h_) + n] = __builtin_amdgcn_mfma_f32_16x16x32_bf16(b, a[m][s_], acc[m][4 * (h_) + n], 0, 0, 0); \
    } \
  } while (0)
#define WAIT_L8() do { asm volatile("s_waitcnt lgkmcnt(8)" ::: "memory"); __builtin_amdgcn_sched_barrier(0); } while (0)
#pragma unroll
  for (int s = 0; s < 16; ++s) {
    if (s <= 10) { WAIT_V(10); } else if (s == 11) { WAIT_V(8); } else if (s == 12) { WAIT_V(6); }
    else if (s == 13) { WAIT_V(4); } else if (s == 14) { WAIT_V(2); } else { WAIT_V(0); }
    BAR();
    K1B_READ(s, 0);
    if (s + 6 < 16) stage(s + 6);
    if (s > 0) { WAIT_L8(); K1B_MMA(s - 1, 1); }
    K1B_READ(s, 1);
    WAIT_L8();
    K1B_MMA(s, 0);
  }
  WAIT_L0();
  K1B_MMA(15, 1);
  STAMP(2);
  float* kvs = (float*)smem;
  __syncthreads();
#pragma unroll
  for (int u = 0; u < 3; ++u) if (tid + u * 512 < 2 * KVP) kvs[tid + u * 512] = kvr[u];
  __syncthreads();
  int tz = threadIdx.x;
  asm volatile("" : "+v"(tz));
  k1b_epilogue(acc, tz & 15, (tz & 63) >> 4, mi, j, wm, wn, wphi + j * 512, kvs, o_out, h_out);
  STAMP(3);
  STAMP_OUT((unsigned long long*)(part + (4 << 20)) + 2048);
}

#define K3_NST 4
__global__ __launch_bounds__(512, 2) void k3(const short* __restrict__ x, const short* __restrict__ o_in,
                                             const short* __restrict__ h_in, const short* __restrict__ wo,
                                             const short* __restrict__ wd, short* __restrict__ out) {
  __shared__ __attribute__((aligned(16))) char smem[K3_NST * 32768];
  const int tid = threadIdx.x, lane = tid & 63, w = __builtin_amdgcn_readfirstlane(tid >> 6);
  const int bid = blockIdx.x, xcd = bid & 7, idx = bid >> 3;
  const int pm = xcd * 4 + (idx & 3), pn = idx >> 2;
  const int wm = w >> 2, wn = w & 3;
  const int lr = lane >> 3, lc = lane & 7;
  const int rr = w * 8 + lr;
  const short* ga_o = o_in + (size_t)(pm * 64 + rr) * DM + 8 * (lc ^ (rr & 7));
  const short* ga_h = h_in + (size_t)(pm * 64 + rr) * FF + 8 * (lc ^ (rr & 7));
  const short* gb_o = wo + (size_t)rr * DM + pn * 64 + 8 * (lc ^ swz128(rr));
  const short* gb_h = wd + (size_t)rr * DM + pn * 64 + 8 * (lc ^ swz128(rr));
  auto stage = [&](int i) {
    char* dst = smem + (i % K3_NST) * 32768 + w * 1024;
#pragma unroll
    for (int u = 0; u < 2; ++u) {
      int t = 2 * i + u;
      glds16((t < 8) ? (ga_o + 64 * t) : (ga_h + 64 * (t - 8)), dst + u * 16384);
      glds16((t < 8) ? (gb_o + (size_t)64 * t * DM) : (gb_h + (size_t)64 * (t - 8) * DM), dst + u * 16384 + 8192);
    }
  };
#pragma unroll
  for (int t = 0; t < K3_NST - 1; ++t) stage(t);
  f32x4 acc[2] = {};
  const int r = lane & 15, g = lane >> 4, q = r >> 2, p = r & 3;
  unsigned aad[2][2], bad[2][2];
#pragma unroll
  for (int kk = 0; kk < 2; ++kk) {
#pragma unroll
    for (int m = 0; m < 2; ++m) {
      int ra = wm * 32 + m * 16 + r;
      aad[kk][m] = lds_addr(smem) + ra * 128 + (((kk * 4 + g) ^ (ra & 7)) << 4);
    }
    int k0 = kk * 32 + 8 * g + q, k1 = k0 + 4, c = wn * 2 + (p >> 1);
    bad[kk][0] = lds_addr(smem) + 8192 + k0 * 128 + ((c ^ swz128(k0)) << 4) + 8 * (p & 1);
    bad[kk][1] = lds_addr(smem) + 8192 + k1 * 128 + ((c ^ swz128(k1)) << 4) + 8 * (p & 1);
  }
  for (int i = 0; i < 18; ++i) {
    if (i < 18 - (K3_NST - 1)) { WAIT_V(8); } else { WAIT_V(0); }
    BAR();
    const unsigned sb = (i % K3_NST) * 32768;
    s16x8 af[2][2][2];
    s16x4 lo[2][2], hi[2][2];
#pragma unroll
    for (int u = 0; u < 2; ++u)
#pragma unroll
      for (int kk = 0; kk < 2; ++kk) {
#pragma unroll
        for (int m = 0; m < 2; ++m)
          asm volatile("ds_read_b128 %0, %1 offset:%2" : "=v"(af[u][kk][m]) : "v"(aad[kk][m] + sb), "i"(u * 16384) : "memory");
        asm volatile("ds_read_b64_tr_b16 %0, %2 offset:%4\n\tds_read_b64_tr_b16 %1, %3 offset:%4"
                     : "=&v"(lo[u][kk]), "=&v"(hi[u][kk]) : "v"(bad[kk][0] + sb), "v"(bad[kk][1] + sb), "i"(u * 16384) : "memory");
      }
    if (i + K3_NST - 1 < 18) stage(i + K3_NST - 1);
#pragma unroll
    for (int u = 0; u < 2; ++u) {
      if (u == 0) { asm volatile("s_waitcnt lgkmcnt(8)" ::: "memory"); __builtin_amdgcn_sched_barrier(0); }
      else WAIT_L0();
#pragma unroll
      for (int kk = 0; kk < 2; ++kk) {
        s16x8 b = __builtin_shufflevector(lo[u][kk], hi[u][kk], 0, 1, 2, 3, 4, 5, 6, 7);
#pragma unroll
        for (int m = 0; m < 2; ++m) acc[m] = __builtin_amdgcn_mfma_f32_16x16x32_bf16(b, af[u][kk][m], acc[m], 0, 0, 0);
      }
    }
  }
#pragma unroll
  for (int m = 0; m < 2; ++m) {
    size_t off = (size_t)(pm * 64 + wm * 32 + m * 16 + r) * DM + pn * 64 + wn * 16 + 4 * g;
    s16x4 xv = *(const s16x4*)(x + off);
    s16x4 ov;
#pragma unroll
    for (int t = 0; t < 4; ++t) ov[t] = f2b(b2f(xv[t]) + acc[m][t]);
    *(s16x4*)(out + off) = ov;
  }
}

extern "C" void kernel_launch(void* const* d_in, const int* in_sizes, int n_in, void* d_out, int out_size,
                              void* d_ws, size_t ws_size, hipStream_t stream) {
  const short* x = (const short*)d_in[0];
  const short* wqkv = (const short*)d_in[1];
  const short* wphi = (const short*)d_in[2];
  const short* wo = (const short*)d_in[3];
  const short* wg = (const short*)d_in[5];
  const short* wu = (const short*)d_in[6];
  const short* wd = (const short*)d_in[7];
  char* ws = (char*)d_ws;
  float* part = (float*)ws;
  short* o_buf = (short*)(ws + (2 << 20));
  short* h_buf = (short*)(ws + (4 << 20));
  short* xn_buf = (short*)(ws + (12 << 20));
  k1a<<<256, 512, 0, stream>>>(x, wqkv, wphi, part, xn_buf);
  k1b<<<256, 512, 0, stream>>>(xn_buf, wqkv, wphi, wg, wu, part, o_buf, h_buf);
  k3<<<256, 512, 0, stream>>>(x, o_buf, h_buf, wo, wd, (short*)d_out);
}
```
